```python
import torch

def _softmax(x, axis=-1):
    return torch.softmax(x, dim=axis)
def _gelu(x):
    return torch.nn.functional.gelu(x, approximate="tanh")
def _silu(x):
    return torch.nn.functional.silu(x)
def _sigmoid(x):
    return torch.sigmoid(x)
def _layernorm(x, axis=-1, eps=1e-5):
    m = x.mean(dim=axis, keepdim=True)
    v = x.var(dim=axis, keepdim=True, unbiased=False)
    return (x - m) / torch.sqrt(v + eps)
def _scatter_add(tgt, idx, src, axis=0):
    return tgt.clone().index_add_(axis, idx, src)
def _segment_sum(src, seg, n):
    out = torch.zeros((n,) + src.shape[1:], dtype=src.dtype)
    return out.index_add_(0, seg, src)
def _pad(x, pw):
    flat = tuple(v for pair in reversed(pw) for v in pair)
    return torch.nn.functional.pad(x, flat)

kernel_name = 'concat_heads__compressed_kv_cla__gqa_block_cached__decode'

def setup_inputs():

    _w = (lambda a: (lambda t: t.to(torch.bfloat16) if t.is_floating_point() else t)(torch.as_tensor(a)))
    def _A_setup():
        B, H, Sq, Sk, D, M = 2, 32, 1, 8192, 128, 512
        Dc = 4 * D
        return {
            "x": _w((lambda *s: torch.randn(*s, dtype=torch.bfloat16) * 3.2)(B, Sq, M)),
            "c_kv": _w((lambda *s: torch.randn(*s, dtype=torch.bfloat16) * 0.1)(B, Sk, Dc)),
            "w_q": _w((lambda *s: torch.randn(*s, dtype=torch.bfloat16) * 6.4)(M, H, D) * (M ** -0.5)),
            "w_uk": _w((lambda *s: torch.randn(*s, dtype=torch.bfloat16) * 6.4)(Dc, H, D) * (Dc ** -0.5)),
            "w_uv": _w((lambda *s: torch.randn(*s, dtype=torch.bfloat16) * 0.1)(Dc, H, D) * (Dc ** -0.5)),
            "w_o": _w((lambda *s: torch.randn(*s, dtype=torch.bfloat16) * 0.1)(H, D, M) * ((H * D) ** -0.5)),
        }
    _A_in = _A_setup()

    def _B_setup():
        B, Sq, D = 2, 1, 512
        Hq, Hkv, Sk, Dh = 32, 8, 8192, 128
        assert Hq % Hkv == 0, (Hq, Hkv)
        return {
            "x": _w((lambda *s: torch.randn(*s, dtype=torch.bfloat16) * 1.6)(B, Sq, D)),
            "k_cache": _w((lambda *s: torch.randn(*s, dtype=torch.bfloat16) * 1.6)(B, Hkv, Sk, Dh)),
            "v_cache": _w((lambda *s: torch.randn(*s, dtype=torch.bfloat16) * 0.05)(B, Hkv, Sk, Dh)),
            "wq": _w((lambda *s: torch.randn(*s, dtype=torch.bfloat16) * 1.6)(D, Hq * Dh) * (D ** -0.5)),
            "wo": _w((lambda *s: torch.randn(*s, dtype=torch.bfloat16) * 0.05)(Hq * Dh, D) * ((Hq * Dh) ** -0.5)),
        }
    _B_in = _B_setup()

    return {
        "A_x": _A_in["x"],
        "A_c_kv": _A_in["c_kv"],
        "A_w_q": _A_in["w_q"],
        "A_w_uk": _A_in["w_uk"],
        "A_w_uv": _A_in["w_uv"],
        "A_w_o": _A_in["w_o"],
        "B_x": _B_in["x"],
        "B_k_cache": _B_in["k_cache"],
        "B_v_cache": _B_in["v_cache"],
        "B_wq": _B_in["wq"],
        "B_wo": _B_in["wo"],
    }

def reference(A_x, A_c_kv, A_w_q, A_w_uk, A_w_uv, A_w_o, B_x, B_k_cache, B_v_cache, B_wq, B_wo):
    def _A_ref(x, c_kv, w_q, w_uk, w_uv, w_o):
        B, Sq, M = x.shape[0], x.shape[1], x.shape[2]
        Sk = c_kv.shape[1]
        H, D = w_q.shape[1], w_q.shape[2]
        q = torch.einsum("bqm,mhd->bhqd", x, w_q)
        k = torch.einsum("bkc,chd->bhkd", c_kv, w_uk)
        v = torch.einsum("bkc,chd->bhkd", c_kv, w_uv)
        scores = torch.einsum("bhqd,bhkd->bhqk", q, k) * (D ** -0.5)
        off = Sk - Sq
        qi = torch.arange(Sq)[:, None]
        kj = torch.arange(Sk)[None, :]
        cmask = kj <= (qi + off)
        scores = torch.where(cmask[None, None], scores, -1e9)
        p = _softmax(scores, axis=-1)
        ctx = torch.einsum("bhqk,bhkd->bhqd", p, v)
        return torch.einsum("bhqd,hdm->bqm", ctx, w_o)

    def _B_ref(x, k_cache, v_cache, wq, wo):
        B, Sq, D = x.shape
        Hq, Dh = 32, 128
        Hkv = k_cache.shape[1]
        rep = Hq // Hkv
        q = (lambda x, *d: x.permute(*d))((x @ wq).reshape(B, Sq, Hq, Dh), 0, 2, 1, 3)
        k = (lambda x, n, axis: torch.repeat_interleave(x, n, dim=axis))(k_cache, rep, axis=1)
        v = (lambda x, n, axis: torch.repeat_interleave(x, n, dim=axis))(v_cache, rep, axis=1)
        scores = torch.einsum("bhqd,bhkd->bhqk", q, k) * (Dh ** -0.5)
        p = _softmax(scores, axis=-1)
        o = torch.einsum("bhqk,bhkd->bhqd", p, v)
        return (lambda x, *d: x.permute(*d))(o, 0, 2, 1, 3).reshape(B, Sq, Hq * Dh) @ wo

    a = _A_ref(A_x, A_c_kv, A_w_q, A_w_uk, A_w_uv, A_w_o)
    b = _B_ref(B_x, B_k_cache, B_v_cache, B_wq, B_wo)
    return torch.concatenate([a, b], axis=-1)
```

```cpp
#include <hip/hip_runtime.h>
#include <hip/hip_bf16.h>
#define NT 1
#define GAS __attribute__((address_space(1)))
#define LAS __attribute__((address_space(3)))
typedef unsigned v4u __attribute__((ext_vector_type(4)));
typedef unsigned v2u __attribute__((ext_vector_type(2)));
typedef float f32x4 __attribute__((ext_vector_type(4)));
typedef float f32x2 __attribute__((ext_vector_type(2)));
typedef float f32x16 __attribute__((ext_vector_type(16)));
typedef __bf16 bf16x8 __attribute__((ext_vector_type(8)));
typedef __bf16 bf16x2 __attribute__((ext_vector_type(2)));
typedef short v4s __attribute__((ext_vector_type(4)));
typedef unsigned short u16;
#define DI __device__ __forceinline__
DI v4u ldg(const void* p) { return NT ? __builtin_nontemporal_load((const GAS v4u*)p) : *(const GAS v4u*)p; }
DI float bflo(unsigned u) { return __builtin_bit_cast(float, u << 16); }
DI float bfhi(unsigned u) { return __builtin_bit_cast(float, u & 0xffff0000u); }
DI unsigned pk(float lo, float hi) { f32x2 v = {lo, hi}; return __builtin_bit_cast(unsigned, __builtin_convertvector(v, bf16x2)); }
DI f32x4 mfma16(v4u a, v4u b, f32x4 c) { return __builtin_amdgcn_mfma_f32_16x16x32_bf16(__builtin_bit_cast(bf16x8, a), __builtin_bit_cast(bf16x8, b), c, 0, 0, 0); }
DI f32x16 mfma32(v4u a, v4u b, f32x16 c) { return __builtin_amdgcn_mfma_f32_32x32x16_bf16(__builtin_bit_cast(bf16x8, a), __builtin_bit_cast(bf16x8, b), c, 0, 0, 0); }
DI float xg_max(float x) {
  auto s = __builtin_amdgcn_permlane16_swap(__float_as_uint(x), __float_as_uint(x), false, false);
  x = fmaxf(__uint_as_float(s[0]), __uint_as_float(s[1]));
  auto t = __builtin_amdgcn_permlane32_swap(__float_as_uint(x), __float_as_uint(x), false, false);
  return fmaxf(__uint_as_float(t[0]), __uint_as_float(t[1]));
}
DI float xg_sum(float x) {
  auto s = __builtin_amdgcn_permlane16_swap(__float_as_uint(x), __float_as_uint(x), false, false);
  x = __uint_as_float(s[0]) + __uint_as_float(s[1]);
  auto t = __builtin_amdgcn_permlane32_swap(__float_as_uint(x), __float_as_uint(x), false, false);
  return __uint_as_float(t[0]) + __uint_as_float(t[1]);
}
constexpr float C2 = 0.088388347648318440f * 1.4426950408889634f;
constexpr size_t WS_Q = 0;
constexpr size_t WS_QABS = 65536;
constexpr size_t WS_BML = 131072 + 65536;
constexpr size_t WS_BO = 262144;
constexpr size_t WS_AML = 1 << 20;
constexpr size_t WS_AO = 2 << 20;
constexpr size_t WS_OP = 16 << 20;

DI void gemv64(const u16* W, const float* xs, float* red, float* res) {
  const int tid = threadIdx.x, cc = tid & 7, rg = tid >> 3;
  v4u w[8];
#pragma unroll
  for (int i = 0; i < 8; ++i) w[i] = ldg(W + (size_t)(rg + 64 * i) * 4096 + cc * 8);
  float a0[8], a1[8];
#pragma unroll
  for (int j = 0; j < 8; ++j) { a0[j] = 0.f; a1[j] = 0.f; }
#pragma unroll
  for (int i = 0; i < 8; ++i) { float x0 = xs[rg + 64 * i], x1 = xs[512 + rg + 64 * i];
#pragma unroll
    for (int j = 0; j < 4; ++j) { float lo = bflo(w[i][j]), hi = bfhi(w[i][j]);
      a0[2 * j] += x0 * lo; a0[2 * j + 1] += x0 * hi; a1[2 * j] += x1 * lo; a1[2 * j + 1] += x1 * hi; } }
#pragma unroll
  for (int j = 0; j < 8; ++j) { red[rg * 129 + cc * 8 + j] = a0[j]; red[rg * 129 + 64 + cc * 8 + j] = a1[j]; }
  __syncthreads();
  if (tid < 128) { float s = 0.f; for (int r = 0; r < 64; ++r) s += red[r * 129 + tid]; res[tid] = s; }
  __syncthreads();
}
__global__ void __launch_bounds__(512) k_q(const u16* xa, const u16* wqa, const u16* xb, const u16* wqb, unsigned char* ws) {
  __shared__ float xs[1024], red[64 * 129], res[128];
  const int br = blockIdx.x >> 6, cg = blockIdx.x & 63, tid = threadIdx.x;
  const u16* x = br ? xb : xa; const u16* W = br ? wqb : wqa;
  xs[tid] = bflo(x[tid]); xs[512 + tid] = bflo(x[512 + tid]);
  __syncthreads();
  gemv64(W + cg * 64, xs, red, res);
  if (tid < 128) ((u16*)(ws + WS_Q))[(br * 2 + (tid >> 6)) * 4096 + cg * 64 + (tid & 63)] = (u16)pk(res[tid], 0.f);
}
__global__ void __launch_bounds__(256) k_abs(const u16* wuk, unsigned char* ws) {
  const int h = blockIdx.x >> 3, cs = blockIdx.x & 7, tid = threadIdx.x, c16 = tid & 15, r = tid >> 4;
  const u16* q = (const u16*)(ws + WS_Q);
  v4u q0 = *(const v4u*)(q + h * 128 + c16 * 8), q1 = *(const v4u*)(q + 4096 + h * 128 + c16 * 8);
#pragma unroll
  for (int i = 0; i < 4; ++i) {
    const int c = cs * 64 + i * 16 + r;
    v4u w = ldg(wuk + (size_t)c * 4096 + h * 128 + c16 * 8);
    float s0 = 0.f, s1 = 0.f;
#pragma unroll
    for (int j = 0; j < 4; ++j) { s0 += bflo(w[j]) * bflo(q0[j]) + bfhi(w[j]) * bfhi(q0[j]); s1 += bflo(w[j]) * bflo(q1[j]) + bfhi(w[j]) * bfhi(q1[j]); }
#pragma unroll
    for (int o = 1; o < 16; o <<= 1) { s0 += __shfl_xor(s0, o); s1 += __shfl_xor(s1, o); }
    if (c16 == 0) { u16* qa = (u16*)(ws + WS_QABS); qa[h * 512 + c] = (u16)pk(s0, 0.f); qa[16384 + h * 512 + c] = (u16)pk(s1, 0.f); }
  }
}
__global__ void __launch_bounds__(512, 2) k_attnB(const u16* K, const u16* V, unsigned char* ws) {
  __shared__ __attribute__((aligned(16))) unsigned char sm[65536 + 1024];
  const int tid = threadIdx.x, lane = tid & 63, w = __builtin_amdgcn_readfirstlane(tid >> 6), c = lane & 15, g = lane >> 4;
  const int pair = blockIdx.x >> 4, sp = blockIdx.x & 15, b = pair >> 3, kvh = pair & 7;
  const size_t base = ((size_t)pair * 8192 + sp * 512 + w * 64) * 128;
  v4u qf[4];
  { const u16* q = (const u16*)(ws + WS_Q) + (2 + b) * 4096 + (kvh * 4 + (c >> 2)) * 128;
#pragma unroll
    for (int s = 0; s < 4; ++s) qf[s] = *(const v4u*)(q + 32 * s + 8 * g); }
  v4u kr[16], vr[16];
#pragma unroll
  for (int j = 0; j < 16; ++j) kr[j] = ldg(K + base + j * 512 + lane * 8);
#pragma unroll
  for (int j = 0; j < 16; ++j) vr[j] = ldg(V + base + j * 512 + lane * 8);
  __builtin_amdgcn_sched_barrier(0);
  unsigned char* my = sm + w * 8192;
  float s[16];
#pragma unroll
  for (int u = 0; u < 2; ++u) {
#pragma unroll
    for (int j = 0; j < 8; ++j) { const int key = 4 * j + g; *(v4u*)(my + key * 256 + ((c ^ (key & 15)) << 4)) = kr[8 * u + j]; }
#pragma unroll
    for (int T = 0; T < 2; ++T) {
      const int key = 16 * T + 4 * (c & 3) + (c >> 2);
      f32x4 d = {0.f, 0.f, 0.f, 0.f};
#pragma unroll
      for (int st = 0; st < 4; ++st) { v4u a = *(const v4u*)(my + key * 256 + (((4 * st + g) ^ (key & 15)) << 4)); d = mfma16(a, qf[st], d); }
#pragma unroll
      for (int r = 0; r < 4; ++r) s[8 * u + 4 * T + r] = d[r] * C2;
    }
  }
  float m = s[0];
#pragma unroll
  for (int i = 1; i < 16; ++i) m = fmaxf(m, s[i]);
  m = xg_max(m);
  float l = 0.f;
#pragma unroll
  for (int i = 0; i < 16; ++i) { s[i] = __builtin_amdgcn_exp2f(s[i] - m); l += s[i]; }
  l = xg_sum(l);
  f32x4 acc[8];
#pragma unroll
  for (int i = 0; i < 8; ++i) acc[i] = f32x4{0.f, 0.f, 0.f, 0.f};
#pragma unroll
  for (int u = 0; u < 2; ++u) {
    v4u p = {pk(s[8 * u], s[8 * u + 1]), pk(s[8 * u + 2], s[8 * u + 3]), pk(s[8 * u + 4], s[8 * u + 5]), pk(s[8 * u + 6], s[8 * u + 7])};
#pragma unroll
    for (int i = 0; i < 8; ++i) {
      v4u bb;
#pragma unroll
      for (int t = 0; t < 4; ++t) bb[t] = __builtin_amdgcn_perm(vr[8 * u + 2 * t + 1][i >> 1], vr[8 * u + 2 * t][i >> 1], (i & 1) ? 0x07060302u : 0x05040100u);
      acc[i] = mfma16(p, bb, acc[i]);
    }
  }
  __syncthreads();
  float* mo = (float*)sm;
  float* ml = (float*)(sm + 65536);
  *(f32x4*)(mo + w * 512 + g * 128 + 8 * c) = f32x4{acc[0][0], acc[1][0], acc[2][0], acc[3][0]};
  *(f32x4*)(mo + w * 512 + g * 128 + 8 * c + 4) = f32x4{acc[4][0], acc[5][0], acc[6][0], acc[7][0]};
  if (g == 0 && (c & 3) == 0) { ml[(w * 4 + (c >> 2)) * 2] = m; ml[(w * 4 + (c >> 2)) * 2 + 1] = l; }
  __syncthreads();
  const int hh = tid >> 7;
  float M = -INFINITY;
#pragma unroll
  for (int i = 0; i < 8; ++i) M = fmaxf(M, ml[(i * 4 + hh) * 2]);
  float L = 0.f, o = 0.f;
#pragma unroll
  for (int i = 0; i < 8; ++i) { float e = __builtin_amdgcn_exp2f(ml[(i * 4 + hh) * 2] - M); L += e * ml[(i * 4 + hh) * 2 + 1]; o += e * mo[i * 512 + tid]; }
  const int row = (b * 32 + kvh * 4 + hh) * 16 + sp;
  ((float*)(ws + WS_BO))[(size_t)row * 128 + (tid & 127)] = o;
  if ((tid & 127) == 0) { ((float*)(ws + WS_BML))[row * 2] = M; ((float*)(ws + WS_BML))[row * 2 + 1] = L; }
}
DI unsigned fsw(unsigned row) { return ((row & 3) << 2) | ((row >> 2) & 3); }
__global__ void __launch_bounds__(512, 2) k_attnA(const u16* ckv, unsigned char* ws) {
  extern __shared__ __attribute__((aligned(16))) unsigned char sm[];
  const int tid = threadIdx.x, lane = tid & 63, w = __builtin_amdgcn_readfirstlane(tid >> 6), c = lane & 15, g = lane >> 4;
  const int b = blockIdx.x >> 6, sp = blockIdx.x & 63;
  const u16* src = ckv + ((size_t)b * 8192 + sp * 128) * 512;
  { v4u t[16];
#pragma unroll
    for (int j = 0; j < 16; ++j) { const int row = w * 16 + j; t[j] = ldg(src + (size_t)row * 512 + ((lane ^ fsw(row)) << 3)); }
#pragma unroll
    for (int j = 0; j < 16; ++j) *(v4u*)(sm + (w * 16 + j) * 1024 + lane * 16) = t[j]; }
  const u16* qa = (const u16*)(ws + WS_QABS) + b * 16384;
  f32x4 d0 = {0.f, 0.f, 0.f, 0.f}, d1 = d0;
  { const int row = w * 16 + c;
#pragma unroll
    for (int s = 0; s < 16; ++s) {
      v4u a = *(const v4u*)(sm + row * 1024 + (((16 * g + s) ^ fsw(row)) << 4));
      v4u b0 = *(const v4u*)(qa + c * 512 + (16 * g + s) * 8), b1 = *(const v4u*)(qa + (16 + c) * 512 + (16 * g + s) * 8);
      d0 = mfma16(a, b0, d0); d1 = mfma16(a, b1, d1);
    } }
  d0 *= C2; d1 *= C2;
  float m0 = xg_max(fmaxf(fmaxf(d0[0], d0[1]), fmaxf(d0[2], d0[3]))), m1 = xg_max(fmaxf(fmaxf(d1[0], d1[1]), fmaxf(d1[2], d1[3])));
  float* mx = (float*)(sm + 131072 + 16384);
  float* ls = mx + 256;
  if (g == 0) { mx[w * 32 + c] = m0; mx[w * 32 + 16 + c] = m1; }
  __syncthreads();
#pragma unroll
  for (int i = 0; i < 8; ++i) { m0 = fmaxf(m0, mx[i * 32 + c]); m1 = fmaxf(m1, mx[i * 32 + 16 + c]); }
  float l0 = 0.f, l1 = 0.f;
#pragma unroll
  for (int r = 0; r < 4; ++r) { d0[r] = __builtin_amdgcn_exp2f(d0[r] - m0); l0 += d0[r]; d1[r] = __builtin_amdgcn_exp2f(d1[r] - m1); l1 += d1[r]; }
  l0 = xg_sum(l0); l1 = xg_sum(l1);
  if (g == 0) { ls[w * 32 + c] = l0; ls[w * 32 + 16 + c] = l1; }
  unsigned char* P = sm + 131072;
  *(v2u*)(P + c * 272 + (16 * w + 4 * g) * 2) = v2u{pk(d0[0], d0[1]), pk(d0[2], d0[3])};
  *(v2u*)(P + (16 + c) * 272 + (16 * w + 4 * g) * 2) = v2u{pk(d1[0], d1[1]), pk(d1[2], d1[3])};
  __syncthreads();
  f32x16 o0, o1;
#pragma unroll
  for (int i = 0; i < 16; ++i) { o0[i] = 0.f; o1[i] = 0.f; }
  { const int hh = lane >> 5, blk = (lane >> 4) & 1, q = (lane & 15) >> 2, p = lane & 3;
#pragma unroll
    for (int ks = 0; ks < 8; ++ks) {
      v4u a = *(const v4u*)(P + (lane & 31) * 272 + (16 * ks + 8 * hh) * 2);
#pragma unroll
      for (int ct = 0; ct < 2; ++ct) {
        v4u bb;
#pragma unroll
        for (int t = 0; t < 2; ++t) {
          const int row = 16 * ks + 8 * hh + 4 * t + q, ch = 8 * w + 4 * ct + 2 * blk + (p >> 1);
          v4s r = __builtin_amdgcn_ds_read_tr16_b64_v4i16((LAS v4s*)(sm + row * 1024 + ((ch ^ fsw(row)) << 4) + 8 * (p & 1)));
          v2u r2 = __builtin_bit_cast(v2u, r); bb[2 * t] = r2[0]; bb[2 * t + 1] = r2[1];
        }
        if (ct == 0) o0 = mfma32(a, bb, o0); else o1 = mfma32(a, bb, o1);
      }
    }
    float* ao = (float*)(ws + WS_AO);
#pragma unroll
    for (int i = 0; i < 16; ++i) { const int h = (i & 3) + 8 * (i >> 2) + 4 * hh;
      float* dst = ao + ((size_t)(b * 32 + h) * 64 + sp) * 512 + 64 * w + (lane & 31);
      dst[0] = o0[i]; dst[32] = o1[i]; }
  }
  if (tid < 32) { float M = -INFINITY, L = 0.f;
    for (int i = 0; i < 8; ++i) { M = fmaxf(M, mx[i * 32 + tid]); L += ls[i * 32 + tid]; }
    float* aml = (float*)(ws + WS_AML) + ((b * 32 + tid) * 64 + sp) * 2; aml[0] = M; aml[1] = L; }
}
DI void gemv_out(const u16* W, const float* y, float* red, float* dst) {
  const int tid = threadIdx.x, lane = tid & 63, w = tid >> 6;
  v4u t[8];
#pragma unroll
  for (int i = 0; i < 8; ++i) t[i] = ldg(W + (size_t)(w * 8 + i) * 512 + lane * 8);
  float a0[8], a1[8];
#pragma unroll
  for (int j = 0; j < 8; ++j) { a0[j] = 0.f; a1[j] = 0.f; }
#pragma unroll
  for (int i = 0; i < 8; ++i) { float x0 = y[w * 8 + i], x1 = y[64 + w * 8 + i];
#pragma unroll
    for (int j = 0; j < 4; ++j) { float lo = bflo(t[i][j]), hi = bfhi(t[i][j]);
      a0[2 * j] += x0 * lo; a0[2 * j + 1] += x0 * hi; a1[2 * j] += x1 * lo; a1[2 * j + 1] += x1 * hi; } }
#pragma unroll
  for (int j = 0; j < 8; ++j) { red[w * 1024 + lane * 8 + j] = a0[j]; red[w * 1024 + 512 + lane * 8 + j] = a1[j]; }
  __syncthreads();
  float s0 = 0.f, s1 = 0.f;
#pragma unroll
  for (int i = 0; i < 8; ++i) { s0 += red[i * 1024 + tid]; s1 += red[i * 1024 + 512 + tid]; }
  dst[tid] = s0; dst[512 + tid] = s1;
}
__global__ void __launch_bounds__(512) k_tail(const u16* wuv, const u16* woa, const u16* wob, unsigned char* ws) {
  __shared__ float xs[1024], red[64 * 129], res[128], wt[128];
  const int br = blockIdx.x >> 6, h = (blockIdx.x & 63) >> 1, dh = blockIdx.x & 1, tid = threadIdx.x;
  float* dst = (float*)(ws + WS_OP) + (size_t)blockIdx.x * 1024;
  if (br == 0) {
    if (tid < 128) { const int b = tid >> 6, s = tid & 63; const float* aml = (const float*)(ws + WS_AML) + ((b * 32 + h) * 64 + s) * 2;
      float m = aml[0], l = aml[1], M = m;
      for (int o = 1; o < 64; o <<= 1) M = fmaxf(M, __shfl_xor(M, o));
      float e = __builtin_amdgcn_exp2f(m - M), L = e * l;
      for (int o = 1; o < 64; o <<= 1) L += __shfl_xor(L, o);
      wt[tid] = e / L; }
    __syncthreads();
    const float* ao = (const float*)(ws + WS_AO);
    float s0 = 0.f, s1 = 0.f;
#pragma unroll 16
    for (int s = 0; s < 64; ++s) { s0 += wt[s] * ao[((size_t)h * 64 + s) * 512 + tid]; s1 += wt[64 + s] * ao[((size_t)(32 + h) * 64 + s) * 512 + tid]; }
    xs[tid] = s0; xs[512 + tid] = s1;
    __syncthreads();
    gemv64(wuv + h * 128 + dh * 64, xs, red, res);
    gemv_out(woa + (size_t)(h * 128 + dh * 64) * 512, res, red, dst);
  } else {
    if (tid < 128) { const int b = tid >> 6, d = tid & 63; const float* bml = (const float*)(ws + WS_BML) + (b * 32 + h) * 32;
      float M = -INFINITY; for (int s = 0; s < 16; ++s) M = fmaxf(M, bml[2 * s]);
      float L = 0.f, o = 0.f;
      for (int s = 0; s < 16; ++s) { float e = __builtin_amdgcn_exp2f(bml[2 * s] - M); L += e * bml[2 * s + 1]; o += e * ((const float*)(ws + WS_BO))[((size_t)(b * 32 + h) * 16 + s) * 128 + dh * 64 + d]; }
      res[tid] = o / L; }
    __syncthreads();
    gemv_out(wob + (size_t)(h * 128 + dh * 64) * 512, res, red, dst);
  }
}
__global__ void __launch_bounds__(64) k_final(unsigned char* ws, u16* out) {
  const int o = blockIdx.x * 64 + threadIdx.x;
  const int br = o >> 10, b = (o >> 9) & 1, col = o & 511;
  const float* p = (const float*)(ws + WS_OP) + (size_t)br * 65536 + b * 512 + col;
  float s = 0.f;
#pragma unroll
  for (int i = 0; i < 64; ++i) s += p[i * 1024];
  out[b * 1024 + br * 512 + col] = (u16)pk(s, 0.f);
}
extern "C" void kernel_launch(void* const* d_in, const int* in_sizes, int n_in, void* d_out, int out_size, void* d_ws, size_t ws_size, hipStream_t stream) {
  const u16* const* in = (const u16* const*)d_in; unsigned char* ws = (unsigned char*)d_ws;
  hipFuncSetAttribute((const void*)k_attnA, hipFuncAttributeMaxDynamicSharedMemorySize, 131072 + 16384 + 2048);
  k_q<<<128, 512, 0, stream>>>(in[0], in[2], in[6], in[9], ws);
  k_abs<<<256, 256, 0, stream>>>(in[3], ws);
  k_attnA<<<128, 512, 131072 + 16384 + 2048, stream>>>(in[1], ws);
  k_attnB<<<256, 512, 0, stream>>>(in[7], in[8], ws);
  k_tail<<<128, 512, 0, stream>>>(in[4], in[5], in[10], ws);
  k_final<<<32, 64, 0, stream>>>(ws, (u16*)d_out);
}
```

```cpp
#include <hip/hip_runtime.h>
#include <hip/hip_bf16.h>
#define NT 1
#define GAS __attribute__((address_space(1)))
#define LAS __attribute__((address_space(3)))
typedef unsigned v4u __attribute__((ext_vector_type(4)));
typedef unsigned v2u __attribute__((ext_vector_type(2)));
typedef float f32x4 __attribute__((ext_vector_type(4)));
typedef float f32x2 __attribute__((ext_vector_type(2)));
typedef float f32x16 __attribute__((ext_vector_type(16)));
typedef __bf16 bf16x8 __attribute__((ext_vector_type(8)));
typedef __bf16 bf16x2 __attribute__((ext_vector_type(2)));
typedef short v4s __attribute__((ext_vector_type(4)));
typedef unsigned short u16;
#define DI __device__ __forceinline__
DI v4u ldg(const void* p) { return NT ? __builtin_nontemporal_load((const GAS v4u*)p) : *(const GAS v4u*)p; }
DI float bflo(unsigned u) { return __builtin_bit_cast(float, u << 16); }
DI float bfhi(unsigned u) { return __builtin_bit_cast(float, u & 0xffff0000u); }
DI unsigned pk(float lo, float hi) { f32x2 v = {lo, hi}; return __builtin_bit_cast(unsigned, __builtin_convertvector(v, bf16x2)); }
DI f32x4 mfma16(v4u a, v4u b, f32x4 c) { return __builtin_amdgcn_mfma_f32_16x16x32_bf16(__builtin_bit_cast(bf16x8, a), __builtin_bit_cast(bf16x8, b), c, 0, 0, 0); }
DI f32x16 mfma32(v4u a, v4u b, f32x16 c) { return __builtin_amdgcn_mfma_f32_32x32x16_bf16(__builtin_bit_cast(bf16x8, a), __builtin_bit_cast(bf16x8, b), c, 0, 0, 0); }
DI float xg_max(float x) {
  auto s = __builtin_amdgcn_permlane16_swap(__float_as_uint(x), __float_as_uint(x), false, false);
  x = fmaxf(__uint_as_float(s[0]), __uint_as_float(s[1]));
  auto t = __builtin_amdgcn_permlane32_swap(__float_as_uint(x), __float_as_uint(x), false, false);
  return fmaxf(__uint_as_float(t[0]), __uint_as_float(t[1]));
}
DI float xg_sum(float x) {
  auto s = __builtin_amdgcn_permlane16_swap(__float_as_uint(x), __float_as_uint(x), false, false);
  x = __uint_as_float(s[0]) + __uint_as_float(s[1]);
  auto t = __builtin_amdgcn_permlane32_swap(__float_as_uint(x), __float_as_uint(x), false, false);
  return __uint_as_float(t[0]) + __uint_as_float(t[1]);
}
constexpr float C2 = 0.088388347648318440f * 1.4426950408889634f;
constexpr size_t WS_Q = 0;
constexpr size_t WS_QABS = 65536;
constexpr size_t WS_BML = 131072 + 65536;
constexpr size_t WS_BO = 262144;
constexpr size_t WS_AML = 1 << 20;
constexpr size_t WS_AO = 2 << 20;
constexpr size_t WS_OP = 16 << 20;

DI void gemv64(const u16* W, const float* xs, float* red, float* res) {
  const int tid = threadIdx.x, cc = tid & 7, rg = tid >> 3;
  v4u w[8];
#pragma unroll
  for (int i = 0; i < 8; ++i) w[i] = ldg(W + (size_t)(rg + 64 * i) * 4096 + cc * 8);
  float a0[8], a1[8];
#pragma unroll
  for (int j = 0; j < 8; ++j) { a0[j] = 0.f; a1[j] = 0.f; }
#pragma unroll
  for (int i = 0; i < 8; ++i) { float x0 = xs[rg + 64 * i], x1 = xs[512 + rg + 64 * i];
#pragma unroll
    for (int j = 0; j < 4; ++j) { float lo = bflo(w[i][j]), hi = bfhi(w[i][j]);
      a0[2 * j] += x0 * lo; a0[2 * j + 1] += x0 * hi; a1[2 * j] += x1 * lo; a1[2 * j + 1] += x1 * hi; } }
#pragma unroll
  for (int j = 0; j < 8; ++j) { red[rg * 129 + cc * 8 + j] = a0[j]; red[rg * 129 + 64 + cc * 8 + j] = a1[j]; }
  __syncthreads();
  if (tid < 128) { float s = 0.f; for (int r = 0; r < 64; ++r) s += red[r * 129 + tid]; res[tid] = s; }
  __syncthreads();
}
__global__ void __launch_bounds__(512) k_q(const u16* xa, const u16* wqa, const u16* xb, const u16* wqb, unsigned char* ws) {
  __shared__ float xs[1024], red[64 * 129], res[128];
  const int br = blockIdx.x >> 6, cg = blockIdx.x & 63, tid = threadIdx.x;
  const u16* x = br ? xb : xa; const u16* W = br ? wqb : wqa;
  xs[tid] = bflo(x[tid]); xs[512 + tid] = bflo(x[512 + tid]);
  __syncthreads();
  gemv64(W + cg * 64, xs, red, res);
  if (tid < 128) ((u16*)(ws + WS_Q))[(br * 2 + (tid >> 6)) * 4096 + cg * 64 + (tid & 63)] = (u16)pk(res[tid], 0.f);
}
__global__ void __launch_bounds__(256) k_abs(const u16* wuk, unsigned char* ws) {
  const int h = blockIdx.x >> 3, cs = blockIdx.x & 7, tid = threadIdx.x, c16 = tid & 15, r = tid >> 4;
  const u16* q = (const u16*)(ws + WS_Q);
  v4u q0 = *(const v4u*)(q + h * 128 + c16 * 8), q1 = *(const v4u*)(q + 4096 + h * 128 + c16 * 8);
#pragma unroll
  for (int i = 0; i < 4; ++i) {
    const int c = cs * 64 + i * 16 + r;
    v4u w = ldg(wuk + (size_t)c * 4096 + h * 128 + c16 * 8);
    float s0 = 0.f, s1 = 0.f;
#pragma unroll
    for (int j = 0; j < 4; ++j) { s0 += bflo(w[j]) * bflo(q0[j]) + bfhi(w[j]) * bfhi(q0[j]); s1 += bflo(w[j]) * bflo(q1[j]) + bfhi(w[j]) * bfhi(q1[j]); }
#pragma unroll
    for (int o = 1; o < 16; o <<= 1) { s0 += __shfl_xor(s0, o); s1 += __shfl_xor(s1, o); }
    if (c16 == 0) { u16* qa = (u16*)(ws + WS_QABS); qa[h * 512 + c] = (u16)pk(s0, 0.f); qa[16384 + h * 512 + c] = (u16)pk(s1, 0.f); }
  }
}
__global__ void __launch_bounds__(512, 2) k_attnB(const u16* K, const u16* V, unsigned char* ws) {
  __shared__ __attribute__((aligned(16))) unsigned char sm[65536 + 1024];
  const int tid = threadIdx.x, lane = tid & 63, w = __builtin_amdgcn_readfirstlane(tid >> 6), c = lane & 15, g = lane >> 4;
  const int pair = blockIdx.x >> 4, sp = blockIdx.x & 15, b = pair >> 3, kvh = pair & 7;
  const size_t base = ((size_t)pair * 8192 + sp * 512 + w * 64) * 128;
  v4u qf[4];
  { const u16* q = (const u16*)(ws + WS_Q) + (2 + b) * 4096 + (kvh * 4 + (c >> 2)) * 128;
#pragma unroll
    for (int s = 0; s < 4; ++s) qf[s] = *(const v4u*)(q + 32 * s + 8 * g); }
  v4u kr[16], vr[16];
#pragma unroll
  for (int j = 0; j < 16; ++j) kr[j] = ldg(K + base + j * 512 + lane * 8);
#pragma unroll
  for (int j = 0; j < 16; ++j) vr[j] = ldg(V + base + j * 512 + lane * 8);
  __builtin_amdgcn_sched_barrier(0);
  unsigned char* my = sm + w * 8192;
  float s[16];
#pragma unroll
  for (int u = 0; u < 2; ++u) {
#pragma unroll
    for (int j = 0; j < 8; ++j) { const int key = 4 * j + g; *(v4u*)(my + key * 256 + ((c ^ (key & 15)) << 4)) = kr[8 * u + j]; }
#pragma unroll
    for (int T = 0; T < 2; ++T) {
      const int key = 16 * T + 4 * (c & 3) + (c >> 2);
      f32x4 d = {0.f, 0.f, 0.f, 0.f};
#pragma unroll
      for (int st = 0; st < 4; ++st) { v4u a = *(const v4u*)(my + key * 256 + (((4 * st + g) ^ (key & 15)) << 4)); d = mfma16(a, qf[st], d); }
#pragma unroll
      for (int r = 0; r < 4; ++r) s[8 * u + 4 * T + r] = d[r] * C2;
    }
  }
  float m = s[0];
#pragma unroll
  for (int i = 1; i < 16; ++i) m = fmaxf(m, s[i]);
  m = xg_max(m);
  float l = 0.f;
#pragma unroll
  for (int i = 0; i < 16; ++i) { s[i] = __builtin_amdgcn_exp2f(s[i] - m); l += s[i]; }
  l = xg_sum(l);
  f32x4 acc[8];
#pragma unroll
  for (int i = 0; i < 8; ++i) acc[i] = f32x4{0.f, 0.f, 0.f, 0.f};
#pragma unroll
  for (int u = 0; u < 2; ++u) {
    v4u p = {pk(s[8 * u], s[8 * u + 1]), pk(s[8 * u + 2], s[8 * u + 3]), pk(s[8 * u + 4], s[8 * u + 5]), pk(s[8 * u + 6], s[8 * u + 7])};
#pragma unroll
    for (int i = 0; i < 8; ++i) {
      v4u bb;
#pragma unroll
      for (int t = 0; t < 4; ++t) bb[t] = __builtin_amdgcn_perm(vr[8 * u + 2 * t + 1][i >> 1], vr[8 * u + 2 * t][i >> 1], (i & 1) ? 0x07060302u : 0x05040100u);
      acc[i] = mfma16(p, bb, acc[i]);
    }
  }
  __syncthreads();
  float* mo = (float*)sm;
  float* ml = (float*)(sm + 65536);
  *(f32x4*)(mo + w * 512 + g * 128 + 8 * c) = f32x4{acc[0][0], acc[1][0], acc[2][0], acc[3][0]};
  *(f32x4*)(mo + w * 512 + g * 128 + 8 * c + 4) = f32x4{acc[4][0], acc[5][0], acc[6][0], acc[7][0]};
  if (g == 0 && (c & 3) == 0) { ml[(w * 4 + (c >> 2)) * 2] = m; ml[(w * 4 + (c >> 2)) * 2 + 1] = l; }
  __syncthreads();
  const int hh = tid >> 7;
  float M = -INFINITY;
#pragma unroll
  for (int i = 0; i < 8; ++i) M = fmaxf(M, ml[(i * 4 + hh) * 2]);
  float L = 0.f, o = 0.f;
#pragma unroll
  for (int i = 0; i < 8; ++i) { float e = __builtin_amdgcn_exp2f(ml[(i * 4 + hh) * 2] - M); L += e * ml[(i * 4 + hh) * 2 + 1]; o += e * mo[i * 512 + tid]; }
  const int row = (b * 32 + kvh * 4 + hh) * 16 + sp;
  ((float*)(ws + WS_BO))[(size_t)row * 128 + (tid & 127)] = o;
  if ((tid & 127) == 0) { ((float*)(ws + WS_BML))[row * 2] = M; ((float*)(ws + WS_BML))[row * 2 + 1] = L; }
}
DI unsigned fsw(unsigned row) { return ((row & 3) << 2) | ((row >> 2) & 3); }
__global__ void __launch_bounds__(512, 2) k_attnA(const u16* ckv, unsigned char* ws) {
  extern __shared__ __attribute__((aligned(16))) unsigned char sm[];
  const int tid = threadIdx.x, lane = tid & 63, w = __builtin_amdgcn_readfirstlane(tid >> 6), c = lane & 15, g = lane >> 4;
  const int b = blockIdx.x >> 6, sp = blockIdx.x & 63;
  const u16* src = ckv + ((size_t)b * 8192 + sp * 128) * 512;
  { v4u t[16];
#pragma unroll
    for (int j = 0; j < 16; ++j) { const int row = w * 16 + j; t[j] = ldg(src + (size_t)row * 512 + ((lane ^ fsw(row)) << 3)); }
#pragma unroll
    for (int j = 0; j < 16; ++j) *(v4u*)(sm + (w * 16 + j) * 1024 + lane * 16) = t[j]; }
  const u16* qa = (const u16*)(ws + WS_QABS) + b * 16384;
  f32x4 d0 = {0.f, 0.f, 0.f, 0.f}, d1 = d0;
  { const int row = w * 16 + c;
#pragma unroll
    for (int s = 0; s < 16; ++s) {
      v4u a = *(const v4u*)(sm + row * 1024 + (((16 * g + s) ^ fsw(row)) << 4));
      v4u b0 = *(const v4u*)(qa + c * 512 + (16 * g + s) * 8), b1 = *(const v4u*)(qa + (16 + c) * 512 + (16 * g + s) * 8);
      d0 = mfma16(a, b0, d0); d1 = mfma16(a, b1, d1);
    } }
  d0 *= C2; d1 *= C2;
  float m0 = xg_max(fmaxf(fmaxf(d0[0], d0[1]), fmaxf(d0[2], d0[3]))), m1 = xg_max(fmaxf(fmaxf(d1[0], d1[1]), fmaxf(d1[2], d1[3])));
  float* mx = (float*)(sm + 131072 + 16384);
  float* ls = mx + 256;
  if (g == 0) { mx[w * 32 + c] = m0; mx[w * 32 + 16 + c] = m1; }
  __syncthreads();
#pragma unroll
  for (int i = 0; i < 8; ++i) { m0 = fmaxf(m0, mx[i * 32 + c]); m1 = fmaxf(m1, mx[i * 32 + 16 + c]); }
  float l0 = 0.f, l1 = 0.f;
#pragma unroll
  for (int r = 0; r < 4; ++r) { d0[r] = __builtin_amdgcn_exp2f(d0[r] - m0); l0 += d0[r]; d1[r] = __builtin_amdgcn_exp2f(d1[r] - m1); l1 += d1[r]; }
  l0 = xg_sum(l0); l1 = xg_sum(l1);
  if (g == 0) { ls[w * 32 + c] = l0; ls[w * 32 + 16 + c] = l1; }
  unsigned char* P = sm + 131072;
  *(v2u*)(P + c * 272 + (16 * w + 4 * g) * 2) = v2u{pk(d0[0], d0[1]), pk(d0[2], d0[3])};
  *(v2u*)(P + (16 + c) * 272 + (16 * w + 4 * g) * 2) = v2u{pk(d1[0], d1[1]), pk(d1[2], d1[3])};
  __syncthreads();
  f32x16 o0, o1;
#pragma unroll
  for (int i = 0; i < 16; ++i) { o0[i] = 0.f; o1[i] = 0.f; }
  { const int hh = lane >> 5, blk = (lane >> 4) & 1, q = (lane & 15) >> 2, p = lane & 3;
#pragma unroll
    for (int ks = 0; ks < 8; ++ks) {
      v4u a = *(const v4u*)(P + (lane & 31) * 272 + (16 * ks + 8 * hh) * 2);
#pragma unroll
      for (int ct = 0; ct < 2; ++ct) {
        v4u bb;
#pragma unroll
        for (int t = 0; t < 2; ++t) {
          const int row = 16 * ks + 8 * hh + 4 * t + q, ch = 8 * w + 4 * ct + 2 * blk + (p >> 1);
          v4s r = __builtin_amdgcn_ds_read_tr16_b64_v4i16((LAS v4s*)(sm + row * 1024 + ((ch ^ fsw(row)) << 4) + 8 * (p & 1)));
          v2u r2 = __builtin_bit_cast(v2u, r); bb[2 * t] = r2[0]; bb[2 * t + 1] = r2[1];
        }
        if (ct == 0) o0 = mfma32(a, bb, o0); else o1 = mfma32(a, bb, o1);
      }
    }
    float* ao = (float*)(ws + WS_AO);
#pragma unroll
    for (int i = 0; i < 16; ++i) { const int h = (i & 3) + 8 * (i >> 2) + 4 * hh;
      float* dst = ao + ((size_t)(b * 32 + h) * 64 + sp) * 512 + 64 * w + (lane & 31);
      dst[0] = o0[i]; dst[32] = o1[i]; }
  }
  if (tid < 32) { float M = -INFINITY, L = 0.f;
    for (int i = 0; i < 8; ++i) { M = fmaxf(M, mx[i * 32 + tid]); L += ls[i * 32 + tid]; }
    float* aml = (float*)(ws + WS_AML) + ((b * 32 + tid) * 64 + sp) * 2; aml[0] = M; aml[1] = L; }
}
DI void gemv_out(const u16* W, const float* y, float* red, float* dst) {
  const int tid = threadIdx.x, lane = tid & 63, w = tid >> 6;
  v4u t[8];
#pragma unroll
  for (int i = 0; i < 8; ++i) t[i] = ldg(W + (size_t)(w * 8 + i) * 512 + lane * 8);
  float a0[8], a1[8];
#pragma unroll
  for (int j = 0; j < 8; ++j) { a0[j] = 0.f; a1[j] = 0.f; }
#pragma unroll
  for (int i = 0; i < 8; ++i) { float x0 = y[w * 8 + i], x1 = y[64 + w * 8 + i];
#pragma unroll
    for (int j = 0; j < 4; ++j) { float lo = bflo(t[i][j]), hi = bfhi(t[i][j]);
      a0[2 * j] += x0 * lo; a0[2 * j + 1] += x0 * hi; a1[2 * j] += x1 * lo; a1[2 * j + 1] += x1 * hi; } }
#pragma unroll
  for (int j = 0; j < 8; ++j) { red[w * 1024 + lane * 8 + j] = a0[j]; red[w * 1024 + 512 + lane * 8 + j] = a1[j]; }
  __syncthreads();
  float s0 = 0.f, s1 = 0.f;
#pragma unroll
  for (int i = 0; i < 8; ++i) { s0 += red[i * 1024 + tid]; s1 += red[i * 1024 + 512 + tid]; }
  dst[tid] = s0; dst[512 + tid] = s1;
}
__global__ void __launch_bounds__(512) k_tail(const u16* wuv, const u16* woa, const u16* wob, unsigned char* ws) {
  __shared__ float xs[1024], red[64 * 129], res[128], wt[128];
  const int br = blockIdx.x >> 6, h = (blockIdx.x & 63) >> 1, dh = blockIdx.x & 1, tid = threadIdx.x;
  float* dst = (float*)(ws + WS_OP) + (size_t)blockIdx.x * 1024;
  if (br == 0) {
    if (tid < 128) { const int b = tid >> 6, s = tid & 63; const float* aml = (const float*)(ws + WS_AML) + ((b * 32 + h) * 64 + s) * 2;
      float m = aml[0], l = aml[1], M = m;
      for (int o = 1; o < 64; o <<= 1) M = fmaxf(M, __shfl_xor(M, o));
      float e = __builtin_amdgcn_exp2f(m - M), L = e * l;
      for (int o = 1; o < 64; o <<= 1) L += __shfl_xor(L, o);
      wt[tid] = e / L; }
    __syncthreads();
    const float* ao = (const float*)(ws + WS_AO);
    float s0 = 0.f, s1 = 0.f;
#pragma unroll 16
    for (int s = 0; s < 64; ++s) { s0 += wt[s] * ao[((size_t)h * 64 + s) * 512 + tid]; s1 += wt[64 + s] * ao[((size_t)(32 + h) * 64 + s) * 512 + tid]; }
    xs[tid] = s0; xs[512 + tid] = s1;
    __syncthreads();
    gemv64(wuv + h * 128 + dh * 64, xs, red, res);
    gemv_out(woa + (size_t)(h * 128 + dh * 64) * 512, res, red, dst);
  } else {
    if (tid < 128) { const int b = tid >> 6, d = tid & 63; const float* bml = (const float*)(ws + WS_BML) + (b * 32 + h) * 32;
      float M = -INFINITY; for (int s = 0; s < 16; ++s) M = fmaxf(M, bml[2 * s]);
      float L = 0.f, o = 0.f;
      for (int s = 0; s < 16; ++s) { float e = __builtin_amdgcn_exp2f(bml[2 * s] - M); L += e * bml[2 * s + 1]; o += e * ((const float*)(ws + WS_BO))[((size_t)(b * 32 + h) * 16 + s) * 128 + dh * 64 + d]; }
      res[tid] = o / L; }
    __syncthreads();
    gemv_out(wob + (size_t)(h * 128 + dh * 64) * 512, res, red, dst);
  }
}
__global__ void __launch_bounds__(64) k_final(unsigned char* ws, u16* out) {
  const int o = blockIdx.x * 64 + threadIdx.x;
  const int br = o >> 10, b = (o >> 9) & 1, col = o & 511;
  const float* p = (const float*)(ws + WS_OP) + (size_t)br * 65536 + b * 512 + col;
  float s = 0.f;
#pragma unroll
  for (int i = 0; i < 64; ++i) s += p[i * 1024];
  out[b * 1024 + br * 512 + col] = (u16)pk(s, 0.f);
}

struct SP { const v4u* p[9]; unsigned* sink; unsigned long long* st; };
template <int SNT> __global__ void __launch_bounds__(512, 2) k_stream(SP a) {
  extern __shared__ char smem[];
  const int c = blockIdx.x, tid = threadIdx.x, lane = tid & 63, w = tid >> 6;
  unsigned long long t0 = __builtin_amdgcn_s_memrealtime();
  v4u v[52];
  int n = 0;
#pragma unroll
  for (int t = 0; t < 9; ++t) {
    const int cnt = t < 6 ? 2 : (t == 6 ? 8 : 16);
    const GAS v4u* p = (const GAS v4u*)a.p[t] + ((size_t)c * 8 + w) * (cnt * 64) + lane;
#pragma unroll
    for (int i = 0; i < cnt; ++i) { v[n++] = SNT ? __builtin_nontemporal_load(p + i * 64) : p[i * 64]; __builtin_amdgcn_sched_barrier(0); }
  }
  unsigned acc = 0;
#pragma unroll
  for (int i = 0; i < 52; ++i) acc ^= v[i].x ^ v[i].y ^ v[i].z ^ v[i].w;
  if (acc == 0x12345678u) a.sink[tid] = acc;
  unsigned long long t1 = __builtin_amdgcn_s_memrealtime();
  if (lane == 0) { a.st[(c * 8 + w) * 2] = t0; a.st[(c * 8 + w) * 2 + 1] = t1; }
}
__global__ void k_amp(unsigned long long* st, int which, int mult) {
  unsigned long long mn = ~0ull, mx = 0;
  for (int i = threadIdx.x; i < 2048; i += 64) { unsigned long long a = st[2 * i], b = st[2 * i + which]; mn = a < mn ? a : mn; mx = b > mx ? b : mx; }
  for (int o = 1; o < 64; o <<= 1) { unsigned long long a = __shfl_xor(mn, o), b = __shfl_xor(mx, o); mn = a < mn ? a : mn; mx = b > mx ? b : mx; }
  unsigned long long d = (mx - mn) * mult; if (d > 200000) d = 200000;
  unsigned long long t0 = __builtin_amdgcn_s_memrealtime();
  while (__builtin_amdgcn_s_memrealtime() - t0 < d) __builtin_amdgcn_s_sleep(1);
}
extern "C" void kernel_launch(void* const* d_in, const int* in_sizes, int n_in, void* d_out, int out_size, void* d_ws, size_t ws_size, hipStream_t stream) {
  const u16* const* in = (const u16* const*)d_in; unsigned char* ws = (unsigned char*)d_ws;
  hipFuncSetAttribute((const void*)k_attnA, hipFuncAttributeMaxDynamicSharedMemorySize, 131072 + 16384 + 2048);
  SP sp; { int idx[9] = {2,3,4,5,9,10,1,7,8}; for (int i = 0; i < 9; ++i) sp.p[i] = (const v4u*)d_in[idx[i]]; sp.sink = (unsigned*)(ws + (64<<20)); sp.st = (unsigned long long*)(ws + (65<<20)); }
  hipFuncSetAttribute((const void*)k_stream<0>, hipFuncAttributeMaxDynamicSharedMemorySize, 96*1024);
  k_stream<0><<<256, 512, 96*1024, stream>>>(sp);
  k_q<<<128, 512, 0, stream>>>(in[0], in[2], in[6], in[9], ws);
  k_abs<<<256, 256, 0, stream>>>(in[3], ws);
  k_attnA<<<128, 512, 131072 + 16384 + 2048, stream>>>(in[1], ws);
  k_attnB<<<256, 512, 0, stream>>>(in[7], in[8], ws);
  k_tail<<<128, 512, 0, stream>>>(in[4], in[5], in[10], ws);
  k_amp<<<1, 64, 0, stream>>>(sp.st, 1, 0);
  k_final<<<32, 64, 0, stream>>>(ws, (u16*)d_out);
}
```

```cpp
#include <hip/hip_runtime.h>
#include <hip/hip_bf16.h>

#define LAS __attribute__((address_space(3)))
#define DI __device__ __forceinline__
typedef unsigned v4u __attribute__((ext_vector_type(4)));
typedef unsigned v2u __attribute__((ext_vector_type(2)));
typedef float f32x4 __attribute__((ext_vector_type(4)));
typedef float f32x2 __attribute__((ext_vector_type(2)));
typedef float f32x16 __attribute__((ext_vector_type(16)));
typedef __bf16 bf16x8 __attribute__((ext_vector_type(8)));
typedef __bf16 bf16x2 __attribute__((ext_vector_type(2)));
typedef short v4s __attribute__((ext_vector_type(4)));
typedef unsigned short u16;
typedef __amdgpu_buffer_rsrc_t rsrc_t;
typedef unsigned lds_t;
#define LP(T, a) ((LAS T*)(size_t)(unsigned)(a))

constexpr int AUX_NT = 2, AUX_SC1 = 16;
constexpr float C2 = 0.088388347648318440f * 1.4426950408889634f;
constexpr unsigned SPIN_CAP = 1u << 16, LSPIN = 1u << 20;

constexpr int WS_EPOCH = 0;
constexpr int WS_QFLAG = 16384;
constexpr int WS_QA = 1572864;
constexpr int WS_QB = 65536;
constexpr int WS_AML = 98304;
constexpr int WS_QABS = 131072;
constexpr int WS_BML = 196608;
constexpr int WS_OPB = 262144;
constexpr int WS_OPA = 524288;
constexpr int WS_BO = 1048576;
constexpr int WS_AO = 2097152;
constexpr int WS_BYTES = 4194304;

struct Params {
  const void *xa, *ckv, *wqa, *wuk, *wuv, *woa, *xb, *kc, *vc, *wqb, *wob;
  void *out, *ws;
};

DI rsrc_t mk(const void* p, int bytes) { return __builtin_amdgcn_make_buffer_rsrc((void*)p, (short)0, bytes, 0x00020000); }
DI v4u ld_nt(rsrc_t r, int voff, int soff) { return __builtin_amdgcn_raw_buffer_load_b128(r, voff, soff, AUX_NT); }
DI v4u ld_sc(rsrc_t r, int voff, int soff) { return __builtin_amdgcn_raw_buffer_load_b128(r, voff, soff, AUX_SC1); }
DI void st_sc(v4u v, rsrc_t r, int voff, int soff) { __builtin_amdgcn_raw_buffer_store_b128(v, r, voff + soff, 0, AUX_SC1); }
DI void dma_nt(rsrc_t r, int voff, int soff, unsigned lds) {
  asm volatile("s_mov_b32 m0, %0\n\ts_nop 0\n\tbuffer_load_dwordx4 %1, %2, %3 offen nt lds" ::"s"(lds), "v"(voff), "s"(r), "s"(soff) : "memory");
}
DI void dma_df(rsrc_t r, int voff, int soff, unsigned lds) {
  asm volatile("s_mov_b32 m0, %0\n\ts_nop 0\n\tbuffer_load_dwordx4 %1, %2, %3 offen lds" ::"s"(lds), "v"(voff), "s"(r), "s"(soff) : "memory");
}
DI void dma_sc(rsrc_t r, int voff, int soff, unsigned lds) {
  asm volatile("s_mov_b32 m0, %0\n\ts_nop 0\n\tbuffer_load_dwordx4 %1, %2, %3 offen sc1 lds" ::"s"(lds), "v"(voff), "s"(r), "s"(soff) : "memory");
}
#define VMCNT(n) asm volatile("s_waitcnt vmcnt(" #n ")" ::: "memory")
template <int N> DI void vmcnt() { asm volatile("s_waitcnt vmcnt(%0)" ::"i"(N) : "memory"); }
#define SB() __builtin_amdgcn_sched_barrier(0)
#define PIN(x) asm volatile("" : "+v"(x))
#define W1 7
#define PFN 10
#define PFWV 1
#define WQK 8
#define WCTL 4
#define WBETA 7
#define WO_EARLY 8
#define WV_S 5
#define FSTR 32
#define QDELAY 0
#define ILV 1
#define QBDELAY 0
#define HOLD 0
#define CODE_KB 38
DI void bar() { asm volatile("s_waitcnt lgkmcnt(0)\n\ts_barrier" ::: "memory"); }
DI float bflo(unsigned u) { return __builtin_bit_cast(float, u << 16); }
DI float bfhi(unsigned u) { return __builtin_bit_cast(float, u & 0xffff0000u); }
DI unsigned fu(float f) { return __builtin_bit_cast(unsigned, f); }
DI float uf(unsigned u) { return __builtin_bit_cast(float, u); }
DI unsigned pk(float lo, float hi) { f32x2 v = {lo, hi}; return __builtin_bit_cast(unsigned, __builtin_convertvector(v, bf16x2)); }
DI float ex2(float x) { return __builtin_amdgcn_exp2f(x); }
DI f32x4 mfma16(v4u a, v4u b, f32x4 c) { return __builtin_amdgcn_mfma_f32_16x16x32_bf16(__builtin_bit_cast(bf16x8, a), __builtin_bit_cast(bf16x8, b), c, 0, 0, 0); }
DI f32x16 mfma32(v4u a, v4u b, f32x16 c) { return __builtin_amdgcn_mfma_f32_32x32x16_bf16(__builtin_bit_cast(bf16x8, a), __builtin_bit_cast(bf16x8, b), c, 0, 0, 0); }
DI v4u lds128(lds_t p) { return *LP(v4u, p); }
DI v2u ldstr(lds_t p) { return __builtin_bit_cast(v2u, __builtin_amdgcn_ds_read_tr16_b64_v4i16(LP(v4s, p))); }
DI float x16_max(float x) { auto s = __builtin_amdgcn_permlane16_swap(fu(x), fu(x), false, false); return fmaxf(uf(s[0]), uf(s[1])); }
DI float x32_max(float x) { auto s = __builtin_amdgcn_permlane32_swap(fu(x), fu(x), false, false); return fmaxf(uf(s[0]), uf(s[1])); }
DI float x16_sum(float x) { auto s = __builtin_amdgcn_permlane16_swap(fu(x), fu(x), false, false); return uf(s[0]) + uf(s[1]); }
DI float x32_sum(float x) { auto s = __builtin_amdgcn_permlane32_swap(fu(x), fu(x), false, false); return uf(s[0]) + uf(s[1]); }
template <int CTRL> DI float dpp(float x) { return uf(__builtin_amdgcn_mov_dpp(fu(x), CTRL, 0xf, 0xf, true)); }
constexpr int XOR1 = 0xB1, XOR2 = 0x4E, ROR4 = 0x124, ROR8 = 0x128;
DI float row_max(float x) { x = fmaxf(x, dpp<XOR1>(x)); x = fmaxf(x, dpp<XOR2>(x)); x = fmaxf(x, dpp<ROR4>(x)); return fmaxf(x, dpp<ROR8>(x)); }
DI float row_sum(float x) { x += dpp<XOR1>(x); x += dpp<XOR2>(x); x += dpp<ROR4>(x); return x + dpp<ROR8>(x); }
DI unsigned fsw(unsigned row) { return ((row & 3) << 2) | ((row >> 2) & 3); }
DI bool tags_ok(v4u v, unsigned tag) { return v[1] == tag && v[3] == tag; }
DI bool all_ok(bool ok) { return __builtin_amdgcn_ballot_w64(!ok) == 0ull; }
DI v4u tfrag(const v4u* r, int i) {
  v4u o;
#pragma unroll
  for (int t = 0; t < 4; ++t) o[t] = __builtin_amdgcn_perm(r[2 * t + 1][i >> 1], r[2 * t][i >> 1], (i & 1) ? 0x07060302u : 0x05040100u);
  return o;
}
DI void transpose8(v4u* r) {
  v4u o[8];
#pragma unroll
  for (int i = 0; i < 8; ++i) o[i] = tfrag(r, i);
#pragma unroll
  for (int i = 0; i < 8; ++i) r[i] = o[i];
}

#define STAMP(i)
#define STAMP_DECL
#define STAMP_FLUSH()

template <int KS> DI void out_gemv(const v4u* wf, lds_t ystage, rsrc_t ws, int dst, int slice_stride, unsigned tag, int lane) {
  const int c = lane & 15, g = lane >> 4;
  v4u a[KS];
#pragma unroll
  for (int t = 0; t < KS; ++t) a[t] = lds128(ystage + ((c >> 2) & 1) * (64 * KS) + 64 * t + 16 * g);
  f32x4 d[8];
#pragma unroll
  for (int i = 0; i < 8; ++i) d[i] = f32x4{0.f, 0.f, 0.f, 0.f};
#pragma unroll
  for (int t = 0; t < KS; ++t)
#pragma unroll
    for (int i = 0; i < 8; ++i) d[i] = mfma16(a[t], wf[8 * t + i], d[i]);
  const bool hi = lane >= 32;
  float v[4];
#pragma unroll
  for (int i = 0; i < 4; ++i) v[i] = hi ? d[4 + i][0] : d[i][0];
  const int voff = (lane >> 3) * slice_stride + (lane & 7) * 16;
  st_sc(v4u{fu(v[0]), tag, fu(v[1]), tag}, ws, voff, dst);
  st_sc(v4u{fu(v[2]), tag, fu(v[3]), tag}, ws, voff, dst + 8 * slice_stride);
}
template <int NL> DI void final_reduce(rsrc_t ws, int src, int slice, void* out, int colbase, unsigned tag, int lane) {
  float s0, s1;
  for (unsigned spins = 0;; ++spins) {
    v4u v[NL];
#pragma unroll
    for (int i = 0; i < NL; ++i) v[i] = ld_sc(ws, lane * 16 + i * 1024, src);
    bool ok = true;
    s0 = 0.f; s1 = 0.f;
#pragma unroll
    for (int i = 0; i < NL; ++i) { ok = ok && tags_ok(v[i], tag); s0 += uf(v[i][0]); s1 += uf(v[i][2]); }
    if (all_ok(ok) || spins >= SPIN_CAP) break;
    asm volatile("" ::: "memory");
  }
  s0 += dpp<ROR8>(s0); s1 += dpp<ROR8>(s1);
  s0 = x32_sum(x16_sum(s0)); s1 = x32_sum(x16_sum(s1));
  if (lane < 8) {
    const int l = 8 * (slice & 7) + lane, c = l & 15, g = l >> 4;
    const int col = 128 * (slice >> 4) + 8 * c + 4 * (g >> 1) + 2 * ((slice >> 3) & 1);
    *(unsigned*)((u16*)out + (g & 1) * 1024 + colbase + col) = pk(s0, s1);
  }
}

DI void role_a(const Params& p, int x, int i, int lane, int w) {
  STAMP_DECL
  STAMP(0);
  const int b = x & 1, sp = (x >> 1) * 8 + i, h = 4 * ((i & 6) + (x & 1)) + (((x >> 1) + (i >> 1)) & 3), dh = i & 1;
  const int c = lane & 15, g = lane >> 4;
  const rsrc_t ws = mk(p.ws, WS_BYTES), rc = mk(p.ckv, 2 * 8192 * 1024);
  constexpr int IMG = 32768;
  const int kbase = (b * 8192 + sp * 256) * 1024;
  auto krow = [&](int row) { return kbase + ((row + 32 * i) & 255) * 1024; };
  if (w == 0) dma_sc(ws, 0, WS_EPOCH, 1024);
  {
    const int v0 = (lane ^ fsw(2 * w)) << 4, v1 = (lane ^ fsw(2 * w + 1)) << 4;
#pragma unroll
    for (int j = 0; j < 16; ++j) { const int row = 2 * w + (j & 1) + 16 * (j >> 1); dma_nt(rc, (j & 1) ? v1 : v0, krow(row), IMG + row * 1024); }
  }
  int ts = c * 1024 + 256 * g + fsw(c) * 16;
  const int hh = lane >> 5, blk = (lane >> 4) & 1, q4 = (lane & 15) >> 2, p4 = lane & 3;
  int ta[2][2];
#pragma unroll
  for (int ct = 0; ct < 2; ++ct)
#pragma unroll
    for (int t = 0; t < 2; ++t) { const int row = 8 * hh + 4 * t + q4; ta[ct][t] = IMG + row * 1024 + (((8 * w + 4 * ct + 2 * blk + (p4 >> 1)) ^ fsw(row)) << 4) + 8 * (p4 & 1); PIN(ta[ct][t]); }
  auto trfrag = [&](int ct, int ks) { const v2u r0 = ldstr(ta[ct][0] + ks * 16384), r1 = ldstr(ta[ct][1] + ks * 16384); return v4u{r0[0], r0[1], r1[0], r1[1]}; };
  int vv[4];
#pragma unroll
  for (int j = 0; j < 4; ++j) vv[j] = (lane ^ fsw(4 * (w & 3) + j)) << 4;
  v4u sa[16], pa[8][2];
  VMCNT(8);
  bar();
  STAMP(1);
  const unsigned tag = __builtin_amdgcn_readfirstlane(*LP(unsigned, 1024)) + 1u;
  if (w < 4) {
#pragma unroll
    for (int s = 0; s < 16; ++s) sa[s] = lds128(IMG + w * 16384 + (ts ^ (s << 4)));
#pragma unroll
    for (int s = 0; s < 16; ++s) PIN(sa[s]);
  }
#pragma unroll
  for (int ks = 0; ks < 4; ++ks) { pa[ks][0] = trfrag(0, ks); pa[ks][1] = trfrag(1, ks); }
#pragma unroll
  for (int ks = 0; ks < 4; ++ks) { PIN(pa[ks][0]); PIN(pa[ks][1]); }
  bar();
  if (w >= 4) {
#pragma unroll
    for (int j = 0; j < 16; ++j) { const int row = 4 * (w - 4) + (j & 3) + 16 * (j >> 2); dma_nt(rc, vv[j & 3], krow(128 + row), IMG + row * 1024); }
    VMCNT(16);
  } else {
    VMCNT(0);
  }
  bar();
  STAMP(2);
  PIN(ts);
  if (w >= 4) {
#pragma unroll
    for (int s = 0; s < 16; ++s) sa[s] = lds128(IMG + w * 16384 + (ts ^ (s << 4)));
#pragma unroll
    for (int s = 0; s < 16; ++s) PIN(sa[s]);
  }
#pragma unroll
  for (int ks = 4; ks < 8; ++ks) { pa[ks][0] = trfrag(0, ks); pa[ks][1] = trfrag(1, ks); }
#pragma unroll
  for (int ks = 4; ks < 8; ++ks) { PIN(pa[ks][0]); PIN(pa[ks][1]); }
  bar();
  STAMP(13);
  if (w >= 4) {
#pragma unroll
    for (int j = 16; j < 32; ++j) { const int row = 4 * (w - 4) + (j & 3) + 16 * (j >> 2); vmcnt<WBETA - 1>(); dma_nt(rc, vv[j & 3], krow(128 + row), IMG + row * 1024); }
    STAMP(3);
  } else if (w == 0) {
    for (int k = 0; k < QDELAY; ++k) __builtin_amdgcn_s_sleep(8);
    for (unsigned spins = 0;; ++spins) {
      const v4u f = ld_sc(ws, (lane >> 1) * FSTR + (lane & 1) * 16, WS_QFLAG);
      if (all_ok(f[0] == tag && f[1] == ~tag && f[2] == tag && f[3] == ~tag) || spins >= SPIN_CAP) break;
      __builtin_amdgcn_s_sleep(2);
    }
    STAMP(3);
#pragma unroll
    for (int j = 0; j < 32; ++j) dma_sc(ws, (lane ^ fsw(j)) << 4, WS_QABS + b * 32768 + j * 1024, j * 1024);
  }
  VMCNT(0);
  bar();
  STAMP(4);
  PIN(ts);
  f32x4 d[2][2];
#pragma unroll
  for (int k = 0; k < 4; ++k) d[k >> 1][k & 1] = f32x4{0.f, 0.f, 0.f, 0.f};
  v4u wv[8], wo[16];
  const rsrc_t rv = mk(p.wuv, 512 * 8192);
  const int vo = (lane >> 3) * 8 * 8192 + (lane & 7) * 16;
  const rsrc_t ro = mk(p.woa, 4096 * 1024);
  auto ld_wo = [&](int n) { return ld_nt(ro, g * 8192 + c * 16, (h * 128 + dh * 64 + 32 * (n >> 3) + (n & 7)) * 1024 + w * 256); };
  {
    v4u q0[3], q1[3], sb[3];
#pragma unroll
    for (int s = 0; s < 18; ++s) {
      if (s < 16) { const int a = ts ^ (s << 4); q0[s % 3] = lds128(a); q1[s % 3] = lds128(a + 16384); sb[s % 3] = lds128(a + (IMG + w * 16384)); }
      SB();
      if (s >= 2) {
        const int t = s - 2;
        d[0][0] = mfma16(sa[t], q0[t % 3], d[0][0]); d[0][1] = mfma16(sa[t], q1[t % 3], d[0][1]);
        d[1][0] = mfma16(sb[t % 3], q0[t % 3], d[1][0]); d[1][1] = mfma16(sb[t % 3], q1[t % 3], d[1][1]);
        { const int k = t < 3 ? t : (t % 3 == 2 ? 2 + t / 3 : -1);
          if (k >= 0) wv[k] = ld_nt(rv, vo, (64 * w + k) * 8192 + h * 256 + dh * 128); }
      }
      SB();
    }
  }
  float m[2];
#pragma unroll
  for (int hg = 0; hg < 2; ++hg) {
    float t = fmaxf(fmaxf(fmaxf(d[0][hg][0], d[0][hg][1]), fmaxf(d[0][hg][2], d[0][hg][3])), fmaxf(fmaxf(d[1][hg][0], d[1][hg][1]), fmaxf(d[1][hg][2], d[1][hg][3])));
    m[hg] = x32_max(x16_max(t));
  }
  constexpr int PST = 528, MX = 17408, LS = 18432, WT = 19456;
  bar();
  STAMP(5);
  if (g < 2) *LP(float, MX + ((16 * g + c) * 8 + w) * 4) = g ? m[1] : m[0];
  bar();
  wv[7] = ld_nt(rv, vo, (64 * w + 7) * 8192 + h * 256 + dh * 128);
#pragma unroll
  for (int hg = 0; hg < 2; ++hg) {
    const f32x4 a0 = *LP(f32x4, MX + (16 * hg + c) * 32), a1 = *LP(f32x4, MX + (16 * hg + c) * 32 + 16);
    m[hg] = fmaxf(fmaxf(fmaxf(a0[0], a0[1]), fmaxf(a0[2], a0[3])), fmaxf(fmaxf(a1[0], a1[1]), fmaxf(a1[2], a1[3])));
  }
  float l[2];
#pragma unroll
  for (int hg = 0; hg < 2; ++hg) {
    const float mc = -m[hg] * C2;
    float s = 0.f;
#pragma unroll
    for (int ab = 0; ab < 2; ++ab) {
      float e[4];
#pragma unroll
      for (int r = 0; r < 4; ++r) { e[r] = ex2(fmaf(d[ab][hg][r], C2, mc)); s += e[r]; }
      *LP(v2u, (16 * hg + c) * PST + (128 * ab + 16 * w + 4 * g) * 2) = v2u{pk(e[0], e[1]), pk(e[2], e[3])};
    }
    l[hg] = x32_sum(x16_sum(s));
  }
  if (g < 2) *LP(float, LS + ((16 * g + c) * 8 + w) * 4) = g ? l[1] : l[0];
  const float Mh = (g & 1) ? m[1] : m[0];
  bar();
  STAMP(6);
  f32x16 o[2];
#pragma unroll
  for (int k = 0; k < 16; ++k) { o[0][k] = 0.f; o[1][k] = 0.f; }
  {
    int pb = (lane & 31) * PST + 16 * hh;
    PIN(pb); PIN(ta[0][0]); PIN(ta[0][1]); PIN(ta[1][0]); PIN(ta[1][1]);
    v4u pf[3], tb[3][2];
#pragma unroll
    for (int s = 0; s < 18; ++s) {
      if (s < 16) { pf[s % 3] = lds128(pb + 32 * s); if (s >= 8) { tb[s % 3][0] = trfrag(0, s - 8); tb[s % 3][1] = trfrag(1, s - 8); } }
      SB();
      if (s >= 2) {
        const int t = s - 2;
        o[0] = mfma32(t < 8 ? pa[t][0] : tb[t % 3][0], pf[t % 3], o[0]);
        o[1] = mfma32(t < 8 ? pa[t][1] : tb[t % 3][1], pf[t % 3], o[1]);
        if (w < 4 && !(t & 1) && (t >> 1) < WO_EARLY) wo[t >> 1] = ld_wo(t >> 1);
      }
      SB();
    }
  }
  float Lh;
  { const f32x4 a0 = *LP(f32x4, LS + (lane & 31) * 32), a1 = *LP(f32x4, LS + (lane & 31) * 32 + 16); Lh = ((a0[0] + a0[1]) + (a0[2] + a0[3])) + ((a1[0] + a1[1]) + (a1[2] + a1[3])); }
  bar();
  STAMP(7);
  {
    const lds_t stg = IMG + w * 4608;
#pragma unroll
    for (int ct = 0; ct < 2; ++ct)
#pragma unroll
      for (int rq = 0; rq < 4; ++rq)
        *LP(v2u, stg + (lane & 31) * 144 + (32 * ct + 8 * rq + 4 * hh) * 2) = v2u{pk(o[ct][4 * rq], o[ct][4 * rq + 1]), pk(o[ct][4 * rq + 2], o[ct][4 * rq + 3])};
#pragma unroll
    for (int e = 0; e < 4; ++e) {
      const v4u v = lds128(stg + (8 * e + (lane >> 3)) * 144 + (lane & 7) * 16);
      st_sc(v, ws, (lane >> 3) * 65536 + (lane & 7) * 16, WS_AO + e * 8 * 65536 + (b * 32 + sp) * 1024 + w * 128);
    }
  }
  VMCNT(0);
  bar();
  STAMP(8);
  if (w == 7 && lane < 32) st_sc(v4u{fu(Mh), tag, fu(Lh), tag}, ws, lane * 16, WS_AML + (b * 32 + sp) * 512);
  if (w < 4) {
#pragma unroll
    for (int n = WO_EARLY; n < 16; ++n) wo[n] = ld_wo(n);
  }
  SB();
  transpose8(wv);
#pragma unroll
  for (int n = 0; n < 8; ++n) PIN(wv[n]);
  constexpr int XS = IMG + 40960, G1 = XS + 8448, YS = G1 + 8192;
  {
    v4u f;
    for (unsigned spins = 0;; ++spins) {
      f = ld_sc(ws, (lane & 7) * 512, WS_AML + w * 4096 + h * 16);
      if (all_ok(tags_ok(f, tag)) || spins >= SPIN_CAP) break;
      __builtin_amdgcn_s_sleep(2);
    }
    asm volatile("" ::: "memory");
    STAMP(9);
    v4u t[8];
#pragma unroll
    for (int k = 0; k < 8; ++k) t[k] = ld_sc(ws, lane * 16 + (k & 3) * 1024, WS_AO + h * 65536 + w * 8192 + (k >> 2) * 4096);
    SB();
    const float mm = uf(f[0]);
    float M = fmaxf(mm, dpp<XOR1>(mm)); M = fmaxf(M, dpp<XOR2>(M)); M = fmaxf(M, dpp<ROR4>(M));
    const float e = ex2((mm - M) * C2);
    float Lw = e * uf(f[2]); Lw += dpp<XOR1>(Lw); Lw += dpp<XOR2>(Lw); Lw += dpp<ROR4>(Lw);
    if (lane == 0) *LP(f32x2, WT + w * 8) = f32x2{M, Lw};
    if (w < 4) { transpose8(wo); transpose8(wo + 8); }
    float xs[8];
#pragma unroll
    for (int j = 0; j < 8; ++j) xs[j] = 0.f;
#pragma unroll
    for (int k = 0; k < 8; ++k) {
      const float wt = uf(__builtin_amdgcn_readlane(fu(e), k));
#pragma unroll
      for (int j = 0; j < 4; ++j) { xs[2 * j] = fmaf(wt, bflo(t[k][j]), xs[2 * j]); xs[2 * j + 1] = fmaf(wt, bfhi(t[k][j]), xs[2 * j + 1]); }
    }
    *LP(v4u, XS + w * 1056 + lane * 16) = v4u{pk(xs[0], xs[1]), pk(xs[2], xs[3]), pk(xs[4], xs[5]), pk(xs[6], xs[7])};
  }
  bar();
  STAMP(10);
  {
    const v4u xa = lds128(XS + (4 * (c >> 3) + (c & 3)) * 1056 + (8 * w + 2 * g + ((c >> 2) & 1)) * 16);
    const f32x4 m0 = *LP(f32x4, WT + hh * 32), m1 = *LP(f32x4, WT + hh * 32 + 16);
    const float M = fmaxf(fmaxf(m0[0], m0[2]), fmaxf(m1[0], m1[2]));
    f32x4 fs = {ex2((m0[0] - M) * C2), ex2((m0[2] - M) * C2), ex2((m1[0] - M) * C2), ex2((m1[2] - M) * C2)};
    fs *= 1.0f / (fs[0] * m0[1] + fs[1] * m0[3] + fs[2] * m1[1] + fs[3] * m1[3]);
    float y[8];
#pragma unroll
    for (int jj = 0; jj < 8; ++jj) { const f32x4 dd = mfma16(xa, wv[jj], f32x4{0.f, 0.f, 0.f, 0.f}); y[jj] = (fs[0] * dd[0] + fs[1] * dd[1]) + (fs[2] * dd[2] + fs[3] * dd[3]); }
    if (((lane >> 3) & 1) == (g & 1)) {
      const lds_t dst = G1 + ((2 * w + (g & 1)) * 128 + (lane >> 5) * 64 + 8 * (lane & 7)) * 4;
      *LP(f32x4, dst) = f32x4{y[0], y[1], y[2], y[3]};
      *LP(f32x4, dst + 16) = f32x4{y[4], y[5], y[6], y[7]};
    }
  }
  bar();
  STAMP(11);
  if (w < 4) {
    f32x2 s = {0.f, 0.f};
#pragma unroll
    for (int k = 0; k < 16; ++k) s += *LP(f32x2, G1 + k * 512 + lane * 8);
    const lds_t ys = YS + w * 256;
    *LP(unsigned, ys + lane * 4) = pk(s[0], s[1]);
    asm volatile("" ::: "memory");
    out_gemv<2>(wo, ys, ws, WS_OPA + w * 16 * 8192 + (2 * h + dh) * 128, 8192, tag, lane);
  } else if (w == 4) {
    const int slice = x * 8 + i;
    final_reduce<8>(ws, WS_OPA + slice * 8192, slice, p.out, 0, tag, lane);
  }
  STAMP(12);
  STAMP_FLUSH();
}

constexpr int B_X = 65536, B_QB = 67584, B_QA = 68608, B_ML = 69120, B_E = 69376, B_CNT = 69632, B_WT = 69888, B_EP = 70656, B_KS = 73728;
#define ST_ARG
#define ST_PASS

DI void k_write(const v4u& kr, int j, lds_t my, int c, int g) { const int key = 4 * j + g; *LP(v4u, my + key * 256 + ((c ^ (key & 15)) << 4)) = kr; }
DI void k_read(v4u* kr, lds_t my, int c, int g) {
  const int k0 = 4 * (c & 3) + (c >> 2);
#pragma unroll
  for (int T = 0; T < 2; ++T)
#pragma unroll
    for (int s = 0; s < 4; ++s) kr[4 * T + s] = lds128(my + (16 * T + k0) * 256 + (((4 * s + g) ^ k0) << 4));
}
DI unsigned lds_arrive(lds_t cnt, int lane) {
  unsigned old = 0;
  asm volatile("s_waitcnt lgkmcnt(0)" ::: "memory");
  if (lane == 0) old = __hip_atomic_fetch_add(LP(unsigned, cnt), 1u, __ATOMIC_RELAXED, __HIP_MEMORY_SCOPE_WORKGROUP);
  old = __builtin_amdgcn_readfirstlane(old);
  asm volatile("" ::: "memory");
  return old;
}
template <int NU> DI void b_scores(const v4u* kr, v4u* pf, rsrc_t ws, int w, int lane, int nwaves, int mlbase, unsigned tag) {
  const int c = lane & 15, g = lane >> 4;
  v4u qf[4];
#pragma unroll
  for (int s = 0; s < 4; ++s) qf[s] = lds128(B_QB + (c >> 2) * 256 + 64 * s + 16 * g);
  float sc[8 * NU];
#pragma unroll
  for (int T = 0; T < 2 * NU; ++T) {
    f32x4 d = {0.f, 0.f, 0.f, 0.f};
#pragma unroll
    for (int s = 0; s < 4; ++s) d = mfma16(kr[4 * T + s], qf[s], d);
#pragma unroll
    for (int r = 0; r < 4; ++r) sc[4 * T + r] = d[r];
  }
  float m = sc[0];
#pragma unroll
  for (int k = 1; k < 8 * NU; ++k) m = fmaxf(m, sc[k]);
  m = x32_max(x16_max(m));
  float l = 0.f;
  const float mc = -m * C2;
#pragma unroll
  for (int k = 0; k < 8 * NU; ++k) { sc[k] = ex2(fmaf(sc[k], C2, mc)); l += sc[k]; }
  l = x32_sum(x16_sum(l));
#pragma unroll
  for (int u = 0; u < NU; ++u)
#pragma unroll
    for (int t = 0; t < 4; ++t) pf[u][t] = pk(sc[8 * u + 2 * t], sc[8 * u + 2 * t + 1]);
  if (lane < 16 && (c & 3) == 0) *LP(f32x2, B_ML + (w * 4 + (c >> 2)) * 8) = f32x2{m, l};
  if ((int)lds_arrive(B_CNT, lane) == nwaves - 1) {
    const f32x2 ml = *LP(f32x2, B_ML + (lane & 31) * 8);
    float M = ml[0];
    M = fmaxf(M, dpp<ROR4>(M)); M = fmaxf(M, dpp<ROR8>(M)); M = x16_max(M);
    const float e = ex2((ml[0] - M) * C2);
    float t = ml[1] * e;
    t += dpp<ROR4>(t); t += dpp<ROR8>(t); t = x16_sum(t);
    if (lane < 32) *LP(float, B_E + ((lane & 3) * 8 + (lane >> 2)) * 4) = e;
    if (lane < 8) st_sc(v4u{fu(M), tag, fu(t), tag}, ws, lane * 16, mlbase);
  }
}
DI void b_pv(const v4u& pf, const v4u* vr, f32x4* acc) {
  v4u t[8];
#pragma unroll
  for (int j = 0; j < 4; ++j) {
#pragma unroll
    for (int k = 0; k < 8; ++k) t[k][j] = __builtin_amdgcn_perm(vr[2 * j + 1][k >> 1], vr[2 * j][k >> 1], (k & 1) ? 0x07060302u : 0x05040100u);
    SB();
  }
#pragma unroll
  for (int k = 0; k < 8; ++k) acc[k] = mfma16(pf, t[k], acc[k]);
#pragma unroll
  for (int k = 0; k < 8; ++k) PIN(acc[k]);
}
DI void b_merge(const f32x4* acc, rsrc_t ws, int w, int lane, int nwaves, int obase, unsigned tag ST_ARG) {
  const lds_t mo = B_KS + w * 8192 + lane * 32;
  *LP(f32x4, mo) = f32x4{acc[0][0], acc[1][0], acc[2][0], acc[3][0]};
  *LP(f32x4, mo + 16) = f32x4{acc[4][0], acc[5][0], acc[6][0], acc[7][0]};
  if ((int)lds_arrive(B_CNT + 4, lane) == nwaves - 1) {
    int ob = B_KS + lane * 16, eb = B_E + (lane >> 5) * 32;
    PIN(ob); PIN(eb);
    f32x4 e[2][2], v[2][8];
#pragma unroll
    for (int eh = 0; eh < 2; ++eh) {
      e[eh][0] = *LP(f32x4, eb + eh * 64); e[eh][1] = *LP(f32x4, eb + eh * 64 + 16);
#pragma unroll
      for (int k = 0; k < 8; ++k) v[eh][k] = *LP(f32x4, ob + k * 8192 + eh * 1024);
    }
    SB();
#pragma unroll
    for (int eh = 0; eh < 2; ++eh) {
      f32x4 s = e[eh][0][0] * v[eh][0];
#pragma unroll
      for (int k = 1; k < 8; ++k) s += e[eh][k >> 2][k & 3] * v[eh][k];
      st_sc(v4u{pk(s[0], s[1]), tag, pk(s[2], s[3]), tag}, ws, (lane >> 5) * 12288 + (lane & 31) * 16, obase + eh * 2 * 12288);
    }
    STAMP(8);
  }
}
DI void lds_signal(lds_t cnt, int lane) { if (lane == 0) (void)__hip_atomic_fetch_add(LP(unsigned, cnt), 1u, __ATOMIC_RELAXED, __HIP_MEMORY_SCOPE_WORKGROUP); }
DI void lds_wait(lds_t cnt, unsigned want) {
  for (unsigned spins = 0; *(volatile LAS unsigned*)(size_t)cnt < want && spins < LSPIN; ++spins) __builtin_amdgcn_s_sleep(1);
  asm volatile("" ::: "memory");
}
template <int NCT> DI void gemv_n(f32x4* d, int ct0, lds_t wb, lds_t xb, int c, int g) {
  const int q4 = c >> 2, p4 = c & 3, fx = (q4 >> 1) | ((g & 1) << 1);
  int a[NCT];
#pragma unroll
  for (int ct = 0; ct < NCT; ++ct) { a[ct] = wb + (8 * g + q4) * 128 + (((ct0 + ct) ^ fx) << 5) + 8 * p4; PIN(a[ct]); }
  int xa = xb + (c & 1) * 512 + 16 * g;
  PIN(xa);
  constexpr int D = NCT == 1 ? 4 : 1;
  v4u xf[D + 1], wf[D + 1][NCT];
#pragma unroll
  for (int s = 0; s < 8 + D; ++s) {
    if (s < 8) {
      xf[s % (D + 1)] = lds128(xa + 64 * s);
#pragma unroll
      for (int ct = 0; ct < NCT; ++ct) { const v2u r0 = ldstr(a[ct] + 4096 * s), r1 = ldstr(a[ct] + 4096 * s + 512); wf[s % (D + 1)][ct] = v4u{r0[0], r0[1], r1[0], r1[1]}; }
    }
    SB();
    if (s >= D) {
#pragma unroll
      for (int ct = 0; ct < NCT; ++ct) d[ct] = mfma16(wf[(s - D) % (D + 1)][ct], xf[(s - D) % (D + 1)], d[ct]);
    }
    SB();
  }
}

template <int NU, int LS, int WIN>
DI void b_stream(rsrc_t rk, rsrc_t rv, rsrc_t ws, int ub, int w, int lane, int nwaves, int obase, int mlbase ST_ARG) {
  const int c = lane & 15, g = lane >> 4;
  const lds_t my = B_KS + w * 8192;
  constexpr int NL = 8 * NU;
  v4u kr[NL], vr[NL], pf[NU];
  f32x4 acc[8];
#pragma unroll
  for (int k = 0; k < 8; ++k) acc[k] = f32x4{0.f, 0.f, 0.f, 0.f};
#pragma unroll
  for (int n = 0; n < NL + WQK; ++n) {
    if (n < WIN) vmcnt<WIN - 1>();
    if (n < NL) kr[n] = ld_nt(rk, lane * 16, ub + n * LS);
    else vr[n - NL] = ld_nt(rv, lane * 16, ub + (n - NL) * LS);
    SB();
    const int t = n + 1 - WIN;
    if (t >= 0 && t < NL) { k_write(kr[t], t & 7, my, c, g); if ((t & 7) == 7) k_read(kr + (t - 7), my, c, g); }
    SB();
  }
  STAMP(3);
  lds_wait(B_CNT + 8, 1u);
  const unsigned tag = __builtin_amdgcn_readfirstlane(*LP(unsigned, B_EP)) + 1u;
  STAMP(4);
  b_scores<NU>(kr, pf, ws, w, lane, nwaves, mlbase, tag);
  SB();
  STAMP(5);
#pragma unroll
  for (int n = WQK; n < NL; ++n) {
    vmcnt<WIN - 1>();
    vr[n] = ld_nt(rv, lane * 16, ub + n * LS);
    SB();
    if (n == 13) { b_pv(pf[0], vr, acc); SB(); }
  }
  STAMP(6);
  if (NU > 1) { b_pv(pf[NU - 2], vr + 8 * (NU - 2), acc); SB(); }
  b_pv(pf[NU - 1], vr + 8 * (NU - 1), acc);
  SB();
  STAMP(7);
  b_merge(acc, ws, w, lane, nwaves, obase, tag ST_PASS);
}
DI void fetch_qb(rsrc_t ws, int src, unsigned tag, int lane) {
  v4u f0, f1;
  for (unsigned spins = 0;; ++spins) {
    f0 = ld_sc(ws, lane * 16, src); f1 = ld_sc(ws, lane * 16 + 1024, src);
    if (all_ok(tags_ok(f0, tag) && tags_ok(f1, tag)) || spins >= SPIN_CAP) break;
    __builtin_amdgcn_s_sleep(4);
  }
  *LP(v2u, B_QB + lane * 8) = v2u{f0[0], f0[2]};
  *LP(v2u, B_QB + 512 + lane * 8) = v2u{f1[0], f1[2]};
  asm volatile("s_waitcnt lgkmcnt(0)" ::: "memory");
  if (lane == 0) *(volatile LAS unsigned*)(size_t)(B_CNT + 8) = 1u;
}

DI void role_b(const Params& p, int x, int j, int lane, int w) {
  STAMP_DECL
  STAMP(0);
  const int type = j % 3, k3 = j / 3, b = j / 12, sp = j % 12;
  const bool tq = type != 2;
  const int c = lane & 15, g = lane >> 4;
  const int col = 8 * k3 + ((x + k3) & 7);
  const rsrc_t ws = mk(p.ws, WS_BYTES);
  const rsrc_t rw = mk(tq ? p.wqa : p.wqb, 512 * 8192);
  const int sb = (type & 1) * 256 * 8192 + col * 128;
  const int vn0 = (lane >> 3) * 8192 + (((lane & 7) ^ ((g & 1) << 1)) << 4), vn1 = vn0 ^ 64;
  auto dma_w = [&](int n) {
    dma_nt(rw, (n & 1) ? vn1 : vn0, sb + ((n >> 2) * 256 + 32 * w + 8 * (n & 3)) * 8192, (n >> 2) * 32768 + (4 * w + (n & 3)) * 1024);
  };
  const int hq = col >> 1, hu = (hq & ~3) | ((hq + 2) & 3), cq = 2 * (col & 1) + type;
  constexpr int DUMMY = 71680;
  if (tq) {
#pragma unroll
    for (int n = 0; n < 4; ++n) dma_w(n);
  } else {
    const int ka = (3 - x - ((unsigned)(size_t)p.wqa >> 7)) & 7, kb = (3 - x - ((unsigned)(size_t)p.wqb >> 7)) & 7;
    const int vp = (lane >> 3) * 8192 + (lane & 7) * 16, sp0 = (8 * k3 + w) * 8 * 8192;
    dma_df(mk(p.wqa, 512 * 8192), vp, sp0 + (8 * ka + ((x + ka) & 7)) * 128, DUMMY);
    dma_df(rw, vp, sp0 + (8 * kb + ((x + kb) & 7)) * 128, DUMMY);
    dma_w(0); dma_w(1);
  }
  if (w == 0) {
    const rsrc_t rx = mk(tq ? p.xa : p.xb, 2048);
    const int vx = (lane >> 5) * 1024 + (lane & 31) * 16;
    dma_df(rx, vx, (type & 1) * 512, B_X); dma_df(rx, vx, 512, B_X + 1024);
    dma_sc(ws, 0, WS_EPOCH, B_EP);
    if (lane < 8) *LP(unsigned, B_CNT + lane * 4) = 0u;
    if (lane < 4) *LP(f32x2, B_ML + lane * 8) = f32x2{-INFINITY, 0.f};
    *LP(f32x4, B_KS + lane * 32) = f32x4{0.f, 0.f, 0.f, 0.f}; *LP(f32x4, B_KS + lane * 32 + 16) = f32x4{0.f, 0.f, 0.f, 0.f};
  }
  const rsrc_t rk = mk(p.kc, 16 * 8192 * 256), rv = mk(p.vc, 16 * 8192 * 256);
  const int pbase = (b * 8 + x) * 8192 * 256;
  const int obase = WS_BO + 4 * x * 12288 + sp * 1024 + b * 512, mlbase = WS_BML + x * 4096 + b * 2048 + sp * 128;
  if (!tq) {
    bar();
#pragma unroll
    for (int n = 2; n < 8; ++n) { VMCNT(4); dma_w(n); }
    VMCNT(4);
    if (w > 0) {
      lds_signal(B_CNT + 12, lane);
      STAMP(1);
      const rsrc_t rc = mk(p.ckv, 2 * 8192 * 1024);
      const int cb = ((x & 1) * 8192 + (x >> 1) * 2048 + k3 * 256) * 1024;
#pragma unroll
      for (int s = 0; s < PFN; ++s) {
        VMCNT(4);
        if (s == 4) { lds_signal(B_CNT + 16, lane); STAMP(2); }
        dma_df(rc, lane * 16, cb + ((256 - 7 * PFN + 7 * s + w - 1 + 32 * k3) & 255) * 1024, DUMMY);
      }
    }
    STAMP(12);
    {
      int cs = 0;
#pragma unroll
      for (int i = 0; i < 8; ++i) { const int cc = 2 * (4 * ((i & 6) + (x & 1)) + (((x >> 1) + (i >> 1)) & 3)) + (i & 1); if (((cc + ((unsigned)(size_t)p.wuv >> 7)) & 7) == 3) cs = cc; }
      if (w > 0) dma_df(mk(p.wuv, 512 * 8192), (lane >> 3) * 8192 + (lane & 7) * 16, (8 * k3 + w) * 8 * 8192 + cs * 128, DUMMY);
    }
  } else {
    bar();
    const rsrc_t ru = mk(p.wuk, 512 * 8192);
    const int vt = g * 8192 + ((c ^ (g | ((w & 1) << 3))) << 4);
#pragma unroll
    for (int n = 0; n < 4; ++n) { VMCNT(3); dma_nt(ru, vt ^ ((n >> 1) << 6), (cq * 128 + 16 * w + 4 * n) * 8192 + hu * 256, 32768 + (4 * w + n) * 1024); }
    VMCNT(4);
    bar();
    STAMP(1);
    if (w >= 4) {
      const unsigned tag = __builtin_amdgcn_readfirstlane(*LP(unsigned, B_EP)) + 1u;
      f32x4 d = {0.f, 0.f, 0.f, 0.f};
      gemv_n<1>(&d, w - 4, 0, B_X, c, g);
      if (c < 4) st_sc(v4u{fu(c < 2 ? d[0] : d[2]), tag, fu(c < 2 ? d[1] : d[3]), tag}, ws, (c & 1) * 1024 + g * 32 + (c >> 1) * 16, WS_QA + hq * 4096 + type * 2048 + (col & 1) * 512 + (w - 4) * 128);
      VMCNT(1);
    } else {
      VMCNT(0);
    }
    bar();
    STAMP(12);
    if (w > 0) { lds_wait(B_CNT + 20, 1u); STAMP(9); }
  }
  if (w > 0) {
    b_stream<3, 7168, W1>(rk, rv, ws, pbase + 21 * sp * 8192 + (w - 1) * 1024, w, lane, tq ? 7 : 8, obase, mlbase ST_PASS);
    STAMP_FLUSH();
    return;
  }
  if (!tq) {
    lds_wait(B_CNT + 12, 7u);
    STAMP(1);
    const unsigned tag = __builtin_amdgcn_readfirstlane(*LP(unsigned, B_EP)) + 1u;
    f32x4 d[4];
#pragma unroll
    for (int ct = 0; ct < 4; ++ct) d[ct] = f32x4{0.f, 0.f, 0.f, 0.f};
#pragma unroll 1
    for (int part = 0; part < 2; ++part) {
      if (part) { VMCNT(0); lds_wait(B_CNT + 16, 7u); STAMP(2); }
      gemv_n<4>(d, 0, part * 32768, B_X + part * 1024, c, g);
    }
    const int ct = (c >> 1) & 3;
    const f32x4 dd = ct == 0 ? d[0] : ct == 1 ? d[1] : ct == 2 ? d[2] : d[3];
    if (c < 8) st_sc(v4u{pk(dd[0], dd[1]), tag, pk(dd[2], dd[3]), tag}, ws, (c & 1) * 16384 + ct * 64 + g * 16, WS_QB + col * 256);
    STAMP(11);
    fetch_qb(ws, WS_QB + (b * 8 + x) * 2048, tag, lane);
    STAMP(13);
    b_stream<1, 1024, WCTL>(rk, rv, ws, pbase + (252 + (k3 & 3)) * 8192, 0, lane, 8, obase, mlbase ST_PASS);
    lds_wait(B_CNT + 4, 8u);
    STAMP(9);
    const int slice = 8 * x + k3;
    final_reduce<4>(ws, WS_OPB + slice * 4096, slice, p.out, 512, tag, lane);
    STAMP(10);
    if (slice == 0) {
      for (unsigned spins = 0;; ++spins) {
        const v4u f = ld_sc(ws, lane * 512, WS_AML);
        if (all_ok(tags_ok(f, tag)) || spins >= SPIN_CAP) break;
        __builtin_amdgcn_s_sleep(1);
      }
      if (lane == 0) __builtin_amdgcn_raw_buffer_store_b32(tag, ws, 0, WS_EPOCH, AUX_SC1);
    }
    STAMP_FLUSH();
    return;
  }
  const unsigned tag = __builtin_amdgcn_readfirstlane(*LP(unsigned, B_EP)) + 1u;
  {
    v4u wf[32];
    {
      const int r0 = 32768 + (8 * (c >> 2) + (c & 3)) * 256;
#pragma unroll
      for (int s = 0; s < 4; ++s) {
        int a = r0 + (((4 * s + g) ^ c) << 4);
        PIN(a);
#pragma unroll
        for (int u = 0; u < 8; ++u) wf[4 * u + s] = lds128(a + (u >> 1) * 8192 + (u & 1) * 1024);
      }
    }
    v4u f[4];
    for (unsigned spins = 0;; ++spins) {
#pragma unroll
      for (int n = 0; n < 4; ++n) f[n] = ld_sc(ws, lane * 16 + n * 1024, WS_QA + hu * 4096);
      if (all_ok(tags_ok(f[0], tag) && tags_ok(f[1], tag) && tags_ok(f[2], tag) && tags_ok(f[3], tag)) || spins >= SPIN_CAP) break;
      asm volatile("" ::: "memory");
    }
    STAMP(2);
    *LP(unsigned, B_QA + lane * 4) = pk(uf(f[0][0]) + uf(f[2][0]), uf(f[0][2]) + uf(f[2][2]));
    *LP(unsigned, B_QA + 256 + lane * 4) = pk(uf(f[1][0]) + uf(f[3][0]), uf(f[1][2]) + uf(f[3][2]));
    v4u qf[4];
#pragma unroll
    for (int s = 0; s < 4; ++s) qf[s] = lds128(B_QA + (c & 1) * 256 + 64 * s + 16 * g);
    f32x4 e[8];
#pragma unroll
    for (int u = 0; u < 8; ++u) e[u] = f32x4{0.f, 0.f, 0.f, 0.f};
#pragma unroll
    for (int s = 0; s < 4; ++s)
#pragma unroll
      for (int u = 0; u < 8; ++u) e[u] = mfma16(wf[4 * u + s], qf[s], e[u]);
    const int ct = (c >> 1) & 3;
    const f32x4 d0 = ct == 0 ? e[0] : ct == 1 ? e[2] : ct == 2 ? e[4] : e[6], d1 = ct == 0 ? e[1] : ct == 1 ? e[3] : ct == 2 ? e[5] : e[7];
    if (c < 8) st_sc(v4u{pk(d0[0], d0[1]), pk(d0[2], d0[3]), pk(d1[0], d1[1]), pk(d1[2], d1[3])}, ws, (c & 1) * 32768 + ct * 64 + g * 16, WS_QABS + hu * 1024 + cq * 256);
    STAMP(3);
    VMCNT(0);
    if (lane == 0) __builtin_amdgcn_raw_buffer_store_b64(v2u{tag, ~tag}, ws, WS_QFLAG + hu * FSTR + cq * 8, 0, AUX_SC1);
    STAMP(4);
    if (lane == 0) *(volatile LAS unsigned*)(size_t)(B_CNT + 20) = 1u;
  }
  for (int k = 0; k < QBDELAY; ++k) __builtin_amdgcn_s_sleep(8);
  STAMP(5);
  fetch_qb(ws, WS_QB + (b * 8 + x) * 2048, tag, lane);
  STAMP(6);
  {
    const int k2 = 2 * k3 + type, h = 4 * x + (k2 >> 2), cg = k2 & 3;
    const rsrc_t ro = mk(p.wob, 4096 * 1024);
    v4u wo[32];
#pragma unroll
    for (int n = 0; n < 32; ++n) { wo[n] = ld_nt(ro, g * 8192 + c * 16, (h * 128 + 32 * (n >> 3) + (n & 7)) * 1024 + cg * 256); vmcnt<WCTL - 1>(); SB(); }
#pragma unroll
    for (int t = 0; t < 4; ++t) transpose8(wo + 8 * t);
#pragma unroll
    for (int n = 0; n < 32; ++n) PIN(wo[n]);
    STAMP(7);
    lds_wait(B_CNT, 7u);
    float wt[12];
    {
      const bool act = (lane & 31) < 12;
      v4u f;
      for (unsigned spins = 0;; ++spins) {
        f = ld_sc(ws, (lane >> 5) * 2048 + (act ? (lane & 31) : 0) * 128, WS_BML + x * 4096 + (h & 3) * 16);
        if (all_ok(tags_ok(f, tag)) || spins >= SPIN_CAP) break;
        __builtin_amdgcn_s_sleep(4);
      }
      const float mm = uf(f[0]), M = row_max(mm);
      const float e = act ? ex2((mm - M) * C2) : 0.f, Lt = row_sum(e * uf(f[2]));
      if ((lane & 16) == 0) *LP(float, B_WT + ((lane >> 5) * 16 + c) * 4) = e / Lt;
      asm volatile("" ::: "memory");
#pragma unroll
      for (int s = 0; s < 3; ++s) {
        const f32x4 t = *LP(f32x4, B_WT + (lane >> 5) * 64 + s * 16);
        wt[4 * s] = t[0]; wt[4 * s + 1] = t[1]; wt[4 * s + 2] = t[2]; wt[4 * s + 3] = t[3];
      }
    }
    STAMP(8);
    lds_wait(B_CNT + 4, 7u);
    float o[4];
    for (unsigned spins = 0;; ++spins) {
      v4u v[12];
#pragma unroll
      for (int s = 0; s < 12; ++s) v[s] = ld_sc(ws, lane * 16 + (s & 3) * 1024, WS_BO + h * 12288 + (s >> 2) * 4096);
      bool ok = true;
      o[0] = o[1] = o[2] = o[3] = 0.f;
#pragma unroll
      for (int s = 0; s < 12; ++s) {
        ok = ok && tags_ok(v[s], tag);
        o[0] = fmaf(wt[s], bflo(v[s][0]), o[0]); o[1] = fmaf(wt[s], bfhi(v[s][0]), o[1]);
        o[2] = fmaf(wt[s], bflo(v[s][2]), o[2]); o[3] = fmaf(wt[s], bfhi(v[s][2]), o[3]);
      }
      if (all_ok(ok) || spins >= SPIN_CAP) break;
      asm volatile("" ::: "memory");
    }
    STAMP(9);
    *LP(v2u, B_QA + lane * 8) = v2u{pk(o[0], o[1]), pk(o[2], o[3])};
    asm volatile("" ::: "memory");
    out_gemv<4>(wo, B_QA, ws, WS_OPB + cg * 16 * 4096 + h * 128, 4096, tag, lane);
    STAMP(10);
  }
  STAMP_FLUSH();
}

__global__ void __launch_bounds__(512, 2) k_fused(Params p) {
  const int lane = threadIdx.x & 63, w = __builtin_amdgcn_readfirstlane(threadIdx.x >> 6);
  const int x = blockIdx.x & 7, i = blockIdx.x >> 3;
  if (w >= 6 && i + 32 * (7 - w) < CODE_KB) {
    const rsrc_t rcode = mk((const void*)(__builtin_amdgcn_s_getpc() & ~255ull), CODE_KB * 1024);
    dma_df(rcode, lane * 16, (i + 32 * (7 - w)) * 1024, i >= 24 ? 2048 : 71680);
  }
  if (i >= 24) role_a(p, x, i - 24, lane, w);
  else role_b(p, x, i, lane, w);
}

extern "C" void kernel_launch(void* const* d_in, const int* in_sizes, int n_in, void* d_out, int out_size, void* d_ws, size_t ws_size, hipStream_t stream) {
  Params p;
  p.xa = d_in[0]; p.ckv = d_in[1]; p.wqa = d_in[2]; p.wuk = d_in[3]; p.wuv = d_in[4]; p.woa = d_in[5];
  p.xb = d_in[6]; p.kc = d_in[7]; p.vc = d_in[8]; p.wqb = d_in[9]; p.wob = d_in[10];
  p.out = d_out; p.ws = d_ws;
  (void)hipFuncSetAttribute((const void*)k_fused, hipFuncAttributeMaxDynamicSharedMemorySize, 163840);
  k_fused<<<256, 512, 163840, stream>>>(p);
}
```

```cpp
#define STAMPS 1
#include <hip/hip_runtime.h>
#include <hip/hip_bf16.h>

#define LAS __attribute__((address_space(3)))
#define DI __device__ __forceinline__
typedef unsigned v4u __attribute__((ext_vector_type(4)));
typedef unsigned v2u __attribute__((ext_vector_type(2)));
typedef float f32x4 __attribute__((ext_vector_type(4)));
typedef float f32x2 __attribute__((ext_vector_type(2)));
typedef float f32x16 __attribute__((ext_vector_type(16)));
typedef __bf16 bf16x8 __attribute__((ext_vector_type(8)));
typedef __bf16 bf16x2 __attribute__((ext_vector_type(2)));
typedef short v4s __attribute__((ext_vector_type(4)));
typedef unsigned short u16;
typedef __amdgpu_buffer_rsrc_t rsrc_t;
typedef unsigned lds_t;
#define LP(T, a) ((LAS T*)(size_t)(unsigned)(a))

constexpr int AUX_NT = 2, AUX_SC1 = 16;
constexpr float C2 = 0.088388347648318440f * 1.4426950408889634f;
constexpr unsigned SPIN_CAP = 1u << 16, LSPIN = 1u << 20;

constexpr int WS_EPOCH = 0;
constexpr int WS_QFLAG = 16384;
constexpr int WS_QA = 1572864;
constexpr int WS_QB = 65536;
constexpr int WS_AML = 98304;
constexpr int WS_QABS = 131072;
constexpr int WS_BML = 196608;
constexpr int WS_OPB = 262144;
constexpr int WS_OPA = 524288;
constexpr int WS_BO = 1048576;
constexpr int WS_AO = 2097152;
constexpr int WS_BYTES = 4194304;
constexpr int WS_ST = 4194304;

struct Params {
  const void *xa, *ckv, *wqa, *wuk, *wuv, *woa, *xb, *kc, *vc, *wqb, *wob;
  void *out, *ws;
};

DI rsrc_t mk(const void* p, int bytes) { return __builtin_amdgcn_make_buffer_rsrc((void*)p, (short)0, bytes, 0x00020000); }
DI v4u ld_nt(rsrc_t r, int voff, int soff) { return __builtin_amdgcn_raw_buffer_load_b128(r, voff, soff, AUX_NT); }
DI v4u ld_sc(rsrc_t r, int voff, int soff) { return __builtin_amdgcn_raw_buffer_load_b128(r, voff, soff, AUX_SC1); }
DI void st_sc(v4u v, rsrc_t r, int voff, int soff) { __builtin_amdgcn_raw_buffer_store_b128(v, r, voff + soff, 0, AUX_SC1); }
DI void dma_nt(rsrc_t r, int voff, int soff, unsigned lds) {
  asm volatile("s_mov_b32 m0, %0\n\ts_nop 0\n\tbuffer_load_dwordx4 %1, %2, %3 offen nt lds" ::"s"(lds), "v"(voff), "s"(r), "s"(soff) : "memory");
}
DI void dma_df(rsrc_t r, int voff, int soff, unsigned lds) {
  asm volatile("s_mov_b32 m0, %0\n\ts_nop 0\n\tbuffer_load_dwordx4 %1, %2, %3 offen lds" ::"s"(lds), "v"(voff), "s"(r), "s"(soff) : "memory");
}
DI void dma_sc(rsrc_t r, int voff, int soff, unsigned lds) {
  asm volatile("s_mov_b32 m0, %0\n\ts_nop 0\n\tbuffer_load_dwordx4 %1, %2, %3 offen sc1 lds" ::"s"(lds), "v"(voff), "s"(r), "s"(soff) : "memory");
}
#define VMCNT(n) asm volatile("s_waitcnt vmcnt(" #n ")" ::: "memory")
template <int N> DI void vmcnt() { asm volatile("s_waitcnt vmcnt(%0)" ::"i"(N) : "memory"); }
#define SB() __builtin_amdgcn_sched_barrier(0)
#define PIN(x) asm volatile("" : "+v"(x))
#define W1 6
#define PFN 10
#define PFWV 1
#define WQK 8
#define WCTL 5
#define WBETA 7
#define WO_EARLY 8
#define WV_S 5
#define FSTR 32
#define QDELAY 0
#define ILV 1
#define QBDELAY 0
#define HOLD 0
#define CODE_KB 38
DI void bar() { asm volatile("s_waitcnt lgkmcnt(0)\n\ts_barrier" ::: "memory"); }
DI float bflo(unsigned u) { return __builtin_bit_cast(float, u << 16); }
DI float bfhi(unsigned u) { return __builtin_bit_cast(float, u & 0xffff0000u); }
DI unsigned fu(float f) { return __builtin_bit_cast(unsigned, f); }
DI float uf(unsigned u) { return __builtin_bit_cast(float, u); }
DI unsigned pk(float lo, float hi) { f32x2 v = {lo, hi}; return __builtin_bit_cast(unsigned, __builtin_convertvector(v, bf16x2)); }
DI float ex2(float x) { return __builtin_amdgcn_exp2f(x); }
DI f32x4 mfma16(v4u a, v4u b, f32x4 c) { return __builtin_amdgcn_mfma_f32_16x16x32_bf16(__builtin_bit_cast(bf16x8, a), __builtin_bit_cast(bf16x8, b), c, 0, 0, 0); }
DI f32x16 mfma32(v4u a, v4u b, f32x16 c) { return __builtin_amdgcn_mfma_f32_32x32x16_bf16(__builtin_bit_cast(bf16x8, a), __builtin_bit_cast(bf16x8, b), c, 0, 0, 0); }
DI v4u lds128(lds_t p) { return *LP(v4u, p); }
DI v2u ldstr(lds_t p) { return __builtin_bit_cast(v2u, __builtin_amdgcn_ds_read_tr16_b64_v4i16(LP(v4s, p))); }
DI float x16_max(float x) { auto s = __builtin_amdgcn_permlane16_swap(fu(x), fu(x), false, false); return fmaxf(uf(s[0]), uf(s[1])); }
DI float x32_max(float x) { auto s = __builtin_amdgcn_permlane32_swap(fu(x), fu(x), false, false); return fmaxf(uf(s[0]), uf(s[1])); }
DI float x16_sum(float x) { auto s = __builtin_amdgcn_permlane16_swap(fu(x), fu(x), false, false); return uf(s[0]) + uf(s[1]); }
DI float x32_sum(float x) { auto s = __builtin_amdgcn_permlane32_swap(fu(x), fu(x), false, false); return uf(s[0]) + uf(s[1]); }
template <int CTRL> DI float dpp(float x) { return uf(__builtin_amdgcn_mov_dpp(fu(x), CTRL, 0xf, 0xf, true)); }
constexpr int XOR1 = 0xB1, XOR2 = 0x4E, ROR4 = 0x124, ROR8 = 0x128;
DI float row_max(float x) { x = fmaxf(x, dpp<XOR1>(x)); x = fmaxf(x, dpp<XOR2>(x)); x = fmaxf(x, dpp<ROR4>(x)); return fmaxf(x, dpp<ROR8>(x)); }
DI float row_sum(float x) { x += dpp<XOR1>(x); x += dpp<XOR2>(x); x += dpp<ROR4>(x); return x + dpp<ROR8>(x); }
DI unsigned fsw(unsigned row) { return ((row & 3) << 2) | ((row >> 2) & 3); }
DI bool tags_ok(v4u v, unsigned tag) { return v[1] == tag && v[3] == tag; }
DI bool all_ok(bool ok) { return __builtin_amdgcn_ballot_w64(!ok) == 0ull; }
DI v4u tfrag(const v4u* r, int i) {
  v4u o;
#pragma unroll
  for (int t = 0; t < 4; ++t) o[t] = __builtin_amdgcn_perm(r[2 * t + 1][i >> 1], r[2 * t][i >> 1], (i & 1) ? 0x07060302u : 0x05040100u);
  return o;
}
DI void transpose8(v4u* r) {
  v4u o[8];
#pragma unroll
  for (int i = 0; i < 8; ++i) o[i] = tfrag(r, i);
#pragma unroll
  for (int i = 0; i < 8; ++i) r[i] = o[i];
}

#define STAMP(i) do { SB(); stv[i] = (unsigned)__builtin_amdgcn_s_memrealtime(); SB(); } while (0)
#define STAMP_DECL unsigned stv[16]; _Pragma("unroll") for (int i_ = 0; i_ < 16; ++i_) stv[i_] = 0; const unsigned clk0_ = (unsigned)__builtin_amdgcn_s_memtime();
#define STAMP_FLUSH() do { { const unsigned c1_ = (unsigned)__builtin_amdgcn_s_memtime(), r1_ = (unsigned)__builtin_amdgcn_s_memrealtime(); stv[15] = stv[0] + (c1_ - clk0_) * 10u / (r1_ - stv[0]); } if (lane == 0) { _Pragma("unroll") for (int i_ = 0; i_ < 16; ++i_) ((unsigned*)((char*)p.ws + WS_ST))[(blockIdx.x * 8 + w) * 16 + i_] = stv[i_]; } } while (0)

template <int KS> DI void out_gemv(const v4u* wf, lds_t ystage, rsrc_t ws, int dst, int slice_stride, unsigned tag, int lane) {
  const int c = lane & 15, g = lane >> 4;
  v4u a[KS];
#pragma unroll
  for (int t = 0; t < KS; ++t) a[t] = lds128(ystage + ((c >> 2) & 1) * (64 * KS) + 64 * t + 16 * g);
  f32x4 d[8];
#pragma unroll
  for (int i = 0; i < 8; ++i) d[i] = f32x4{0.f, 0.f, 0.f, 0.f};
#pragma unroll
  for (int t = 0; t < KS; ++t)
#pragma unroll
    for (int i = 0; i < 8; ++i) d[i] = mfma16(a[t], wf[8 * t + i], d[i]);
  const bool hi = lane >= 32;
  float v[4];
#pragma unroll
  for (int i = 0; i < 4; ++i) v[i] = hi ? d[4 + i][0] : d[i][0];
  const int voff = (lane >> 3) * slice_stride + (lane & 7) * 16;
  st_sc(v4u{fu(v[0]), tag, fu(v[1]), tag}, ws, voff, dst);
  st_sc(v4u{fu(v[2]), tag, fu(v[3]), tag}, ws, voff, dst + 8 * slice_stride);
}
template <int NL> DI void final_reduce(rsrc_t ws, int src, int slice, void* out, int colbase, unsigned tag, int lane) {
  float s0, s1;
  for (unsigned spins = 0;; ++spins) {
    v4u v[NL];
#pragma unroll
    for (int i = 0; i < NL; ++i) v[i] = ld_sc(ws, lane * 16 + i * 1024, src);
    bool ok = true;
    s0 = 0.f; s1 = 0.f;
#pragma unroll
    for (int i = 0; i < NL; ++i) { ok = ok && tags_ok(v[i], tag); s0 += uf(v[i][0]); s1 += uf(v[i][2]); }
    if (all_ok(ok) || spins >= SPIN_CAP) break;
    asm volatile("" ::: "memory");
  }
  s0 += dpp<ROR8>(s0); s1 += dpp<ROR8>(s1);
  s0 = x32_sum(x16_sum(s0)); s1 = x32_sum(x16_sum(s1));
  if (lane < 8) {
    const int l = 8 * (slice & 7) + lane, c = l & 15, g = l >> 4;
    const int col = 128 * (slice >> 4) + 8 * c + 4 * (g >> 1) + 2 * ((slice >> 3) & 1);
    *(unsigned*)((u16*)out + (g & 1) * 1024 + colbase + col) = pk(s0, s1);
  }
}

DI void role_a(const Params& p, int x, int i, int lane, int w) {
  STAMP_DECL
  STAMP(0);
  const int b = x & 1, sp = (x >> 1) * 8 + i, h = 4 * ((i & 6) + (x & 1)) + (((x >> 1) + (i >> 1)) & 3), dh = i & 1;
  const int c = lane & 15, g = lane >> 4;
  const rsrc_t ws = mk(p.ws, WS_BYTES), rc = mk(p.ckv, 2 * 8192 * 1024);
  constexpr int IMG = 32768;
  const int kbase = (b * 8192 + sp * 256) * 1024;
  auto krow = [&](int row) { return kbase + ((row + 32 * i) & 255) * 1024; };
  if (w == 0) dma_sc(ws, 0, WS_EPOCH, 1024);
  {
    const int v0 = (lane ^ fsw(2 * w)) << 4, v1 = (lane ^ fsw(2 * w + 1)) << 4;
#pragma unroll
    for (int j = 0; j < 16; ++j) { const int row = 2 * w + (j & 1) + 16 * (j >> 1); dma_nt(rc, (j & 1) ? v1 : v0, krow(row), IMG + row * 1024); }
  }
  int ts = c * 1024 + 256 * g + fsw(c) * 16;
  const int hh = lane >> 5, blk = (lane >> 4) & 1, q4 = (lane & 15) >> 2, p4 = lane & 3;
  int ta[2][2];
#pragma unroll
  for (int ct = 0; ct < 2; ++ct)
#pragma unroll
    for (int t = 0; t < 2; ++t) { const int row = 8 * hh + 4 * t + q4; ta[ct][t] = IMG + row * 1024 + (((8 * w + 4 * ct + 2 * blk + (p4 >> 1)) ^ fsw(row)) << 4) + 8 * (p4 & 1); PIN(ta[ct][t]); }
  auto trfrag = [&](int ct, int ks) { const v2u r0 = ldstr(ta[ct][0] + ks * 16384), r1 = ldstr(ta[ct][1] + ks * 16384); return v4u{r0[0], r0[1], r1[0], r1[1]}; };
  int vv[4];
#pragma unroll
  for (int j = 0; j < 4; ++j) vv[j] = (lane ^ fsw(4 * (w & 3) + j)) << 4;
  v4u sa[16], pa[8][2];
  VMCNT(8);
  bar();
  STAMP(1);
  const unsigned tag = __builtin_amdgcn_readfirstlane(*LP(unsigned, 1024)) + 1u;
  if (w < 4) {
#pragma unroll
    for (int s = 0; s < 16; ++s) sa[s] = lds128(IMG + w * 16384 + (ts ^ (s << 4)));
#pragma unroll
    for (int s = 0; s < 16; ++s) PIN(sa[s]);
  }
#pragma unroll
  for (int ks = 0; ks < 4; ++ks) { pa[ks][0] = trfrag(0, ks); pa[ks][1] = trfrag(1, ks); }
#pragma unroll
  for (int ks = 0; ks < 4; ++ks) { PIN(pa[ks][0]); PIN(pa[ks][1]); }
  bar();
  if (w >= 4) {
#pragma unroll
    for (int j = 0; j < 16; ++j) { const int row = 4 * (w - 4) + (j & 3) + 16 * (j >> 2); dma_nt(rc, vv[j & 3], krow(128 + row), IMG + row * 1024); }
    VMCNT(16);
  } else {
    VMCNT(0);
  }
  bar();
  STAMP(2);
  PIN(ts);
  if (w >= 4) {
#pragma unroll
    for (int s = 0; s < 16; ++s) sa[s] = lds128(IMG + w * 16384 + (ts ^ (s << 4)));
#pragma unroll
    for (int s = 0; s < 16; ++s) PIN(sa[s]);
  }
#pragma unroll
  for (int ks = 4; ks < 8; ++ks) { pa[ks][0] = trfrag(0, ks); pa[ks][1] = trfrag(1, ks); }
#pragma unroll
  for (int ks = 4; ks < 8; ++ks) { PIN(pa[ks][0]); PIN(pa[ks][1]); }
  bar();
  STAMP(13);
  if (w >= 4) {
#pragma unroll
    for (int j = 16; j < 32; ++j) { const int row = 4 * (w - 4) + (j & 3) + 16 * (j >> 2); vmcnt<WBETA - 1>(); dma_nt(rc, vv[j & 3], krow(128 + row), IMG + row * 1024); }
    STAMP(3);
  } else if (w == 0) {
    for (int k = 0; k < QDELAY; ++k) __builtin_amdgcn_s_sleep(8);
    for (unsigned spins = 0;; ++spins) {
      const v4u f = ld_sc(ws, (lane >> 1) * FSTR + (lane & 1) * 16, WS_QFLAG);
      if (all_ok(f[0] == tag && f[1] == ~tag && f[2] == tag && f[3] == ~tag) || spins >= SPIN_CAP) break;
      __builtin_amdgcn_s_sleep(2);
    }
    STAMP(3);
#pragma unroll
    for (int j = 0; j < 32; ++j) dma_sc(ws, (lane ^ fsw(j)) << 4, WS_QABS + b * 32768 + j * 1024, j * 1024);
  }
  VMCNT(0);
  bar();
  STAMP(4);
  PIN(ts);
  f32x4 d[2][2];
#pragma unroll
  for (int k = 0; k < 4; ++k) d[k >> 1][k & 1] = f32x4{0.f, 0.f, 0.f, 0.f};
  v4u wv[8], wo[16];
  const rsrc_t rv = mk(p.wuv, 512 * 8192);
  const int vo = (lane >> 3) * 8 * 8192 + (lane & 7) * 16;
  const rsrc_t ro = mk(p.woa, 4096 * 1024);
  auto ld_wo = [&](int n) { return ld_nt(ro, g * 8192 + c * 16, (h * 128 + dh * 64 + 32 * (n >> 3) + (n & 7)) * 1024 + w * 256); };
  {
    v4u q0[3], q1[3], sb[3];
#pragma unroll
    for (int s = 0; s < 18; ++s) {
      if (s < 16) { const int a = ts ^ (s << 4); q0[s % 3] = lds128(a); q1[s % 3] = lds128(a + 16384); sb[s % 3] = lds128(a + (IMG + w * 16384)); }
      SB();
      if (s >= 2) {
        const int t = s - 2;
        d[0][0] = mfma16(sa[t], q0[t % 3], d[0][0]); d[0][1] = mfma16(sa[t], q1[t % 3], d[0][1]);
        d[1][0] = mfma16(sb[t % 3], q0[t % 3], d[1][0]); d[1][1] = mfma16(sb[t % 3], q1[t % 3], d[1][1]);
        { const int k = t < 3 ? t : (t % 3 == 2 ? 2 + t / 3 : -1);
          if (k >= 0) wv[k] = ld_nt(rv, vo, (64 * w + k) * 8192 + h * 256 + dh * 128); }
      }
      SB();
    }
  }
  float m[2];
#pragma unroll
  for (int hg = 0; hg < 2; ++hg) {
    float t = fmaxf(fmaxf(fmaxf(d[0][hg][0], d[0][hg][1]), fmaxf(d[0][hg][2], d[0][hg][3])), fmaxf(fmaxf(d[1][hg][0], d[1][hg][1]), fmaxf(d[1][hg][2], d[1][hg][3])));
    m[hg] = x32_max(x16_max(t));
  }
  constexpr int PST = 528, MX = 17408, LS = 18432, WT = 19456;
  bar();
  STAMP(5);
  if (g < 2) *LP(float, MX + ((16 * g + c) * 8 + w) * 4) = g ? m[1] : m[0];
  bar();
  wv[7] = ld_nt(rv, vo, (64 * w + 7) * 8192 + h * 256 + dh * 128);
#pragma unroll
  for (int hg = 0; hg < 2; ++hg) {
    const f32x4 a0 = *LP(f32x4, MX + (16 * hg + c) * 32), a1 = *LP(f32x4, MX + (16 * hg + c) * 32 + 16);
    m[hg] = fmaxf(fmaxf(fmaxf(a0[0], a0[1]), fmaxf(a0[2], a0[3])), fmaxf(fmaxf(a1[0], a1[1]), fmaxf(a1[2], a1[3])));
  }
  float l[2];
#pragma unroll
  for (int hg = 0; hg < 2; ++hg) {
    const float mc = -m[hg] * C2;
    float s = 0.f;
#pragma unroll
    for (int ab = 0; ab < 2; ++ab) {
      float e[4];
#pragma unroll
      for (int r = 0; r < 4; ++r) { e[r] = ex2(fmaf(d[ab][hg][r], C2, mc)); s += e[r]; }
      *LP(v2u, (16 * hg + c) * PST + (128 * ab + 16 * w + 4 * g) * 2) = v2u{pk(e[0], e[1]), pk(e[2], e[3])};
    }
    l[hg] = x32_sum(x16_sum(s));
  }
  if (g < 2) *LP(float, LS + ((16 * g + c) * 8 + w) * 4) = g ? l[1] : l[0];
  const float Mh = (g & 1) ? m[1] : m[0];
  bar();
  STAMP(6);
  f32x16 o[2];
#pragma unroll
  for (int k = 0; k < 16; ++k) { o[0][k] = 0.f; o[1][k] = 0.f; }
  {
    int pb = (lane & 31) * PST + 16 * hh;
    PIN(pb); PIN(ta[0][0]); PIN(ta[0][1]); PIN(ta[1][0]); PIN(ta[1][1]);
    v4u pf[3], tb[3][2];
#pragma unroll
    for (int s = 0; s < 18; ++s) {
      if (s < 16) { pf[s % 3] = lds128(pb + 32 * s); if (s >= 8) { tb[s % 3][0] = trfrag(0, s - 8); tb[s % 3][1] = trfrag(1, s - 8); } }
      SB();
      if (s >= 2) {
        const int t = s - 2;
        o[0] = mfma32(t < 8 ? pa[t][0] : tb[t % 3][0], pf[t % 3], o[0]);
        o[1] = mfma32(t < 8 ? pa[t][1] : tb[t % 3][1], pf[t % 3], o[1]);
        if (w < 4 && !(t & 1) && (t >> 1) < WO_EARLY) wo[t >> 1] = ld_wo(t >> 1);
      }
      SB();
    }
  }
  float Lh;
  { const f32x4 a0 = *LP(f32x4, LS + (lane & 31) * 32), a1 = *LP(f32x4, LS + (lane & 31) * 32 + 16); Lh = ((a0[0] + a0[1]) + (a0[2] + a0[3])) + ((a1[0] + a1[1]) + (a1[2] + a1[3])); }
  bar();
  STAMP(7);
  {
    const lds_t stg = IMG + w * 4608;
#pragma unroll
    for (int ct = 0; ct < 2; ++ct)
#pragma unroll
      for (int rq = 0; rq < 4; ++rq)
        *LP(v2u, stg + (lane & 31) * 144 + (32 * ct + 8 * rq + 4 * hh) * 2) = v2u{pk(o[ct][4 * rq], o[ct][4 * rq + 1]), pk(o[ct][4 * rq + 2], o[ct][4 * rq + 3])};
#pragma unroll
    for (int e = 0; e < 4; ++e) {
      const v4u v = lds128(stg + (8 * e + (lane >> 3)) * 144 + (lane & 7) * 16);
      st_sc(v, ws, (lane >> 3) * 65536 + (lane & 7) * 16, WS_AO + e * 8 * 65536 + (b * 32 + sp) * 1024 + w * 128);
    }
  }
  VMCNT(0);
  bar();
  STAMP(8);
  if (w == 7 && lane < 32) st_sc(v4u{fu(Mh), tag, fu(Lh), tag}, ws, lane * 16, WS_AML + (b * 32 + sp) * 512);
  if (w < 4) {
#pragma unroll
    for (int n = WO_EARLY; n < 16; ++n) wo[n] = ld_wo(n);
  }
  SB();
  transpose8(wv);
#pragma unroll
  for (int n = 0; n < 8; ++n) PIN(wv[n]);
  constexpr int XS = IMG + 40960, G1 = XS + 8448, YS = G1 + 8192;
  {
    v4u f;
    for (unsigned spins = 0;; ++spins) {
      f = ld_sc(ws, (lane & 7) * 512, WS_AML + w * 4096 + h * 16);
      if (all_ok(tags_ok(f, tag)) || spins >= SPIN_CAP) break;
      __builtin_amdgcn_s_sleep(2);
    }
    asm volatile("" ::: "memory");
    STAMP(9);
    v4u t[8];
#pragma unroll
    for (int k = 0; k < 8; ++k) t[k] = ld_sc(ws, lane * 16 + (k & 3) * 1024, WS_AO + h * 65536 + w * 8192 + (k >> 2) * 4096);
    SB();
    const float mm = uf(f[0]);
    float M = fmaxf(mm, dpp<XOR1>(mm)); M = fmaxf(M, dpp<XOR2>(M)); M = fmaxf(M, dpp<ROR4>(M));
    const float e = ex2((mm - M) * C2);
    float Lw = e * uf(f[2]); Lw += dpp<XOR1>(Lw); Lw += dpp<XOR2>(Lw); Lw += dpp<ROR4>(Lw);
    if (lane == 0) *LP(f32x2, WT + w * 8) = f32x2{M, Lw};
    if (w < 4) { transpose8(wo); transpose8(wo + 8); }
    float xs[8];
#pragma unroll
    for (int j = 0; j < 8; ++j) xs[j] = 0.f;
#pragma unroll
    for (int k = 0; k < 8; ++k) {
      const float wt = uf(__builtin_amdgcn_readlane(fu(e), k));
#pragma unroll
      for (int j = 0; j < 4; ++j) { xs[2 * j] = fmaf(wt, bflo(t[k][j]), xs[2 * j]); xs[2 * j + 1] = fmaf(wt, bfhi(t[k][j]), xs[2 * j + 1]); }
    }
    *LP(v4u, XS + w * 1056 + lane * 16) = v4u{pk(xs[0], xs[1]), pk(xs[2], xs[3]), pk(xs[4], xs[5]), pk(xs[6], xs[7])};
  }
  bar();
  STAMP(10);
  {
    const v4u xa = lds128(XS + (4 * (c >> 3) + (c & 3)) * 1056 + (8 * w + 2 * g + ((c >> 2) & 1)) * 16);
    const f32x4 m0 = *LP(f32x4, WT + hh * 32), m1 = *LP(f32x4, WT + hh * 32 + 16);
    const float M = fmaxf(fmaxf(m0[0], m0[2]), fmaxf(m1[0], m1[2]));
    f32x4 fs = {ex2((m0[0] - M) * C2), ex2((m0[2] - M) * C2), ex2((m1[0] - M) * C2), ex2((m1[2] - M) * C2)};
    fs *= 1.0f / (fs[0] * m0[1] + fs[1] * m0[3] + fs[2] * m1[1] + fs[3] * m1[3]);
    float y[8];
#pragma unroll
    for (int jj = 0; jj < 8; ++jj) { const f32x4 dd = mfma16(xa, wv[jj], f32x4{0.f, 0.f, 0.f, 0.f}); y[jj] = (fs[0] * dd[0] + fs[1] * dd[1]) + (fs[2] * dd[2] + fs[3] * dd[3]); }
    if (((lane >> 3) & 1) == (g & 1)) {
      const lds_t dst = G1 + ((2 * w + (g & 1)) * 128 + (lane >> 5) * 64 + 8 * (lane & 7)) * 4;
      *LP(f32x4, dst) = f32x4{y[0], y[1], y[2], y[3]};
      *LP(f32x4, dst + 16) = f32x4{y[4], y[5], y[6], y[7]};
    }
  }
  bar();
  STAMP(11);
  if (w < 4) {
    f32x2 s = {0.f, 0.f};
#pragma unroll
    for (int k = 0; k < 16; ++k) s += *LP(f32x2, G1 + k * 512 + lane * 8);
    const lds_t ys = YS + w * 256;
    *LP(unsigned, ys + lane * 4) = pk(s[0], s[1]);
    asm volatile("" ::: "memory");
    out_gemv<2>(wo, ys, ws, WS_OPA + w * 16 * 8192 + (2 * h + dh) * 128, 8192, tag, lane);
  } else if (w == 4) {
    const int slice = x * 8 + i;
    final_reduce<8>(ws, WS_OPA + slice * 8192, slice, p.out, 0, tag, lane);
  }
  STAMP(12);
  STAMP_FLUSH();
}

constexpr int B_X = 65536, B_QB = 67584, B_QA = 68608, B_ML = 69120, B_E = 69376, B_CNT = 69632, B_WT = 69888, B_EP = 70656, B_KS = 73728;
#define ST_ARG , unsigned* stv
#define ST_PASS , stv

DI void k_write(const v4u& kr, int j, lds_t my, int c, int g) { const int key = 4 * j + g; *LP(v4u, my + key * 256 + ((c ^ (key & 15)) << 4)) = kr; }
DI void k_read(v4u* kr, lds_t my, int c, int g) {
  const int k0 = 4 * (c & 3) + (c >> 2);
#pragma unroll
  for (int T = 0; T < 2; ++T)
#pragma unroll
    for (int s = 0; s < 4; ++s) kr[4 * T + s] = lds128(my + (16 * T + k0) * 256 + (((4 * s + g) ^ k0) << 4));
}
DI unsigned lds_arrive(lds_t cnt, int lane) {
  unsigned old = 0;
  asm volatile("s_waitcnt lgkmcnt(0)" ::: "memory");
  if (lane == 0) old = __hip_atomic_fetch_add(LP(unsigned, cnt), 1u, __ATOMIC_RELAXED, __HIP_MEMORY_SCOPE_WORKGROUP);
  old = __builtin_amdgcn_readfirstlane(old);
  asm volatile("" ::: "memory");
  return old;
}
template <int NU> DI void b_scores(const v4u* kr, v4u* pf, rsrc_t ws, int w, int lane, int nwaves, int mlbase, unsigned tag) {
  const int c = lane & 15, g = lane >> 4;
  v4u qf[4];
#pragma unroll
  for (int s = 0; s < 4; ++s) qf[s] = lds128(B_QB + (c >> 2) * 256 + 64 * s + 16 * g);
  float sc[8 * NU];
#pragma unroll
  for (int T = 0; T < 2 * NU; ++T) {
    f32x4 d = {0.f, 0.f, 0.f, 0.f};
#pragma unroll
    for (int s = 0; s < 4; ++s) d = mfma16(kr[4 * T + s], qf[s], d);
#pragma unroll
    for (int r = 0; r < 4; ++r) sc[4 * T + r] = d[r];
  }
  float m = sc[0];
#pragma unroll
  for (int k = 1; k < 8 * NU; ++k) m = fmaxf(m, sc[k]);
  m = x32_max(x16_max(m));
  float l = 0.f;
  const float mc = -m * C2;
#pragma unroll
  for (int k = 0; k < 8 * NU; ++k) { sc[k] = ex2(fmaf(sc[k], C2, mc)); l += sc[k]; }
  l = x32_sum(x16_sum(l));
#pragma unroll
  for (int u = 0; u < NU; ++u)
#pragma unroll
    for (int t = 0; t < 4; ++t) pf[u][t] = pk(sc[8 * u + 2 * t], sc[8 * u + 2 * t + 1]);
  if (lane < 16 && (c & 3) == 0) *LP(f32x2, B_ML + (w * 4 + (c >> 2)) * 8) = f32x2{m, l};
  if ((int)lds_arrive(B_CNT, lane) == nwaves - 1) {
    const f32x2 ml = *LP(f32x2, B_ML + (lane & 31) * 8);
    float M = ml[0];
    M = fmaxf(M, dpp<ROR4>(M)); M = fmaxf(M, dpp<ROR8>(M)); M = x16_max(M);
    const float e = ex2((ml[0] - M) * C2);
    float t = ml[1] * e;
    t += dpp<ROR4>(t); t += dpp<ROR8>(t); t = x16_sum(t);
    if (lane < 32) *LP(float, B_E + ((lane & 3) * 8 + (lane >> 2)) * 4) = e;
    if (lane < 8) st_sc(v4u{fu(M), tag, fu(t), tag}, ws, lane * 16, mlbase);
  }
}
DI void b_pv(const v4u& pf, const v4u* vr, f32x4* acc) {
  v4u t[8];
#pragma unroll
  for (int j = 0; j < 4; ++j) {
#pragma unroll
    for (int k = 0; k < 8; ++k) t[k][j] = __builtin_amdgcn_perm(vr[2 * j + 1][k >> 1], vr[2 * j][k >> 1], (k & 1) ? 0x07060302u : 0x05040100u);
    SB();
  }
#pragma unroll
  for (int k = 0; k < 8; ++k) acc[k] = mfma16(pf, t[k], acc[k]);
#pragma unroll
  for (int k = 0; k < 8; ++k) PIN(acc[k]);
}
DI void b_merge(const f32x4* acc, rsrc_t ws, int w, int lane, int nwaves, int obase, unsigned tag ST_ARG) {
  const lds_t mo = B_KS + w * 8192 + lane * 32;
  *LP(f32x4, mo) = f32x4{acc[0][0], acc[1][0], acc[2][0], acc[3][0]};
  *LP(f32x4, mo + 16) = f32x4{acc[4][0], acc[5][0], acc[6][0], acc[7][0]};
  if ((int)lds_arrive(B_CNT + 4, lane) == nwaves - 1) {
    int ob = B_KS + lane * 16, eb = B_E + (lane >> 5) * 32;
    PIN(ob); PIN(eb);
    f32x4 e[2][2], v[2][8];
#pragma unroll
    for (int eh = 0; eh < 2; ++eh) {
      e[eh][0] = *LP(f32x4, eb + eh * 64); e[eh][1] = *LP(f32x4, eb + eh * 64 + 16);
#pragma unroll
      for (int k = 0; k < 8; ++k) v[eh][k] = *LP(f32x4, ob + k * 8192 + eh * 1024);
    }
    SB();
#pragma unroll
    for (int eh = 0; eh < 2; ++eh) {
      f32x4 s = e[eh][0][0] * v[eh][0];
#pragma unroll
      for (int k = 1; k < 8; ++k) s += e[eh][k >> 2][k & 3] * v[eh][k];
      st_sc(v4u{pk(s[0], s[1]), tag, pk(s[2], s[3]), tag}, ws, (lane >> 5) * 12288 + (lane & 31) * 16, obase + eh * 2 * 12288);
    }
    STAMP(8);
  }
}
DI void lds_signal(lds_t cnt, int lane) { if (lane == 0) (void)__hip_atomic_fetch_add(LP(unsigned, cnt), 1u, __ATOMIC_RELAXED, __HIP_MEMORY_SCOPE_WORKGROUP); }
DI void lds_wait(lds_t cnt, unsigned want) {
  for (unsigned spins = 0; *(volatile LAS unsigned*)(size_t)cnt < want && spins < LSPIN; ++spins) __builtin_amdgcn_s_sleep(1);
  asm volatile("" ::: "memory");
}
template <int NCT> DI void gemv_n(f32x4* d, int ct0, lds_t wb, lds_t xb, int c, int g) {
  const int q4 = c >> 2, p4 = c & 3, fx = (q4 >> 1) | ((g & 1) << 1);
  int a[NCT];
#pragma unroll
  for (int ct = 0; ct < NCT; ++ct) { a[ct] = wb + (8 * g + q4) * 128 + (((ct0 + ct) ^ fx) << 5) + 8 * p4; PIN(a[ct]); }
  int xa = xb + (c & 1) * 512 + 16 * g;
  PIN(xa);
  constexpr int D = NCT == 1 ? 4 : 1;
  v4u xf[D + 1], wf[D + 1][NCT];
#pragma unroll
  for (int s = 0; s < 8 + D; ++s) {
    if (s < 8) {
      xf[s % (D + 1)] = lds128(xa + 64 * s);
#pragma unroll
      for (int ct = 0; ct < NCT; ++ct) { const v2u r0 = ldstr(a[ct] + 4096 * s), r1 = ldstr(a[ct] + 4096 * s + 512); wf[s % (D + 1)][ct] = v4u{r0[0], r0[1], r1[0], r1[1]}; }
    }
    SB();
    if (s >= D) {
#pragma unroll
      for (int ct = 0; ct < NCT; ++ct) d[ct] = mfma16(wf[(s - D) % (D + 1)][ct], xf[(s - D) % (D + 1)], d[ct]);
    }
    SB();
  }
}

template <int NU, int LS, int WIN>
DI void b_stream(rsrc_t rk, rsrc_t rv, rsrc_t ws, int ub, int w, int lane, int nwaves, int obase, int mlbase ST_ARG) {
  const int c = lane & 15, g = lane >> 4;
  const lds_t my = B_KS + w * 8192;
  constexpr int NL = 8 * NU;
  v4u kr[NL], vr[NL], pf[NU];
  f32x4 acc[8];
#pragma unroll
  for (int k = 0; k < 8; ++k) acc[k] = f32x4{0.f, 0.f, 0.f, 0.f};
#pragma unroll
  for (int n = 0; n < NL + WQK; ++n) {
    if (n < WIN) vmcnt<WIN - 1>();
    if (n < NL) kr[n] = ld_nt(rk, lane * 16, ub + n * LS);
    else vr[n - NL] = ld_nt(rv, lane * 16, ub + (n - NL) * LS);
    SB();
    const int t = n + 1 - WIN;
    if (t >= 0 && t < NL) { k_write(kr[t], t & 7, my, c, g); if ((t & 7) == 7) k_read(kr + (t - 7), my, c, g); }
    SB();
  }
  STAMP(3);
  lds_wait(B_CNT + 8, 1u);
  const unsigned tag = __builtin_amdgcn_readfirstlane(*LP(unsigned, B_EP)) + 1u;
  STAMP(4);
  b_scores<NU>(kr, pf, ws, w, lane, nwaves, mlbase, tag);
  SB();
  STAMP(5);
#pragma unroll
  for (int n = WQK; n < NL; ++n) {
    vmcnt<WIN - 1>();
    vr[n] = ld_nt(rv, lane * 16, ub + n * LS);
    SB();
    if (n == 13) { b_pv(pf[0], vr, acc); SB(); }
  }
  STAMP(6);
  if (NU > 1) { b_pv(pf[NU - 2], vr + 8 * (NU - 2), acc); SB(); }
  b_pv(pf[NU - 1], vr + 8 * (NU - 1), acc);
  SB();
  STAMP(7);
  b_merge(acc, ws, w, lane, nwaves, obase, tag ST_PASS);
}
DI void fetch_qb(rsrc_t ws, int src, unsigned tag, int lane) {
  v4u f0, f1;
  for (unsigned spins = 0;; ++spins) {
    f0 = ld_sc(ws, lane * 16, src); f1 = ld_sc(ws, lane * 16 + 1024, src);
    if (all_ok(tags_ok(f0, tag) && tags_ok(f1, tag)) || spins >= SPIN_CAP) break;
    __builtin_amdgcn_s_sleep(4);
  }
  *LP(v2u, B_QB + lane * 8) = v2u{f0[0], f0[2]};
  *LP(v2u, B_QB + 512 + lane * 8) = v2u{f1[0], f1[2]};
  asm volatile("s_waitcnt lgkmcnt(0)" ::: "memory");
  if (lane == 0) *(volatile LAS unsigned*)(size_t)(B_CNT + 8) = 1u;
}

DI void role_b(const Params& p, int x, int j, int lane, int w) {
  STAMP_DECL
  STAMP(0);
  const int type = j % 3, k3 = j / 3, b = j / 12, sp = j % 12;
  const bool tq = type != 2;
  const int c = lane & 15, g = lane >> 4;
  const int col = 8 * k3 + ((x + k3) & 7);
  const rsrc_t ws = mk(p.ws, WS_BYTES);
  const rsrc_t rw = mk(tq ? p.wqa : p.wqb, 512 * 8192);
  const int sb = (type & 1) * 256 * 8192 + col * 128;
  const int vn0 = (lane >> 3) * 8192 + (((lane & 7) ^ ((g & 1) << 1)) << 4), vn1 = vn0 ^ 64;
  auto dma_w = [&](int n) {
    dma_nt(rw, (n & 1) ? vn1 : vn0, sb + ((n >> 2) * 256 + 32 * w + 8 * (n & 3)) * 8192, (n >> 2) * 32768 + (4 * w + (n & 3)) * 1024);
  };
  const int hq = col >> 1, hu = (hq & ~3) | ((hq + 2) & 3), cq = 2 * (col & 1) + type;
  constexpr int DUMMY = 71680;
  if (tq) {
#pragma unroll
    for (int n = 0; n < 4; ++n) dma_w(n);
  } else {
    const int ka = (3 - x - ((unsigned)(size_t)p.wqa >> 7)) & 7, kb = (3 - x - ((unsigned)(size_t)p.wqb >> 7)) & 7;
    const int vp = (lane >> 3) * 8192 + (lane & 7) * 16, sp0 = (8 * k3 + w) * 8 * 8192;
    dma_df(mk(p.wqa, 512 * 8192), vp, sp0 + (8 * ka + ((x + ka) & 7)) * 128, DUMMY);
    dma_df(rw, vp, sp0 + (8 * kb + ((x + kb) & 7)) * 128, DUMMY);
    dma_w(0); dma_w(1);
  }
  if (w == 0) {
    const rsrc_t rx = mk(tq ? p.xa : p.xb, 2048);
    const int vx = (lane >> 5) * 1024 + (lane & 31) * 16;
    dma_df(rx, vx, (type & 1) * 512, B_X); dma_df(rx, vx, 512, B_X + 1024);
    dma_sc(ws, 0, WS_EPOCH, B_EP);
    if (lane < 8) *LP(unsigned, B_CNT + lane * 4) = 0u;
    if (lane < 4) *LP(f32x2, B_ML + lane * 8) = f32x2{-INFINITY, 0.f};
    *LP(f32x4, B_KS + lane * 32) = f32x4{0.f, 0.f, 0.f, 0.f}; *LP(f32x4, B_KS + lane * 32 + 16) = f32x4{0.f, 0.f, 0.f, 0.f};
  }
  const rsrc_t rk = mk(p.kc, 16 * 8192 * 256), rv = mk(p.vc, 16 * 8192 * 256);
  const int pbase = (b * 8 + x) * 8192 * 256;
  const int obase = WS_BO + 4 * x * 12288 + sp * 1024 + b * 512, mlbase = WS_BML + x * 4096 + b * 2048 + sp * 128;
  if (!tq) {
    bar();
#pragma unroll
    for (int n = 2; n < 8; ++n) { VMCNT(4); dma_w(n); }
    VMCNT(4);
    if (w > 0) {
      lds_signal(B_CNT + 12, lane);
      STAMP(1);
      const rsrc_t rc = mk(p.ckv, 2 * 8192 * 1024);
      const int cb = ((x & 1) * 8192 + (x >> 1) * 2048 + k3 * 256) * 1024;
#pragma unroll
      for (int s = 0; s < PFN; ++s) {
        VMCNT(4);
        if (s == 4) { lds_signal(B_CNT + 16, lane); STAMP(2); }
        dma_df(rc, lane * 16, cb + ((256 - 7 * PFN + 7 * s + w - 1 + 32 * k3) & 255) * 1024, DUMMY);
      }
    }
    STAMP(12);
    {
      int cs = 0;
#pragma unroll
      for (int i = 0; i < 8; ++i) { const int cc = 2 * (4 * ((i & 6) + (x & 1)) + (((x >> 1) + (i >> 1)) & 3)) + (i & 1); if (((cc + ((unsigned)(size_t)p.wuv >> 7)) & 7) == 3) cs = cc; }
      if (w > 0) dma_df(mk(p.wuv, 512 * 8192), (lane >> 3) * 8192 + (lane & 7) * 16, (8 * k3 + w) * 8 * 8192 + cs * 128, DUMMY);
    }
  } else {
    bar();
    const rsrc_t ru = mk(p.wuk, 512 * 8192);
    const int vt = g * 8192 + ((c ^ (g | ((w & 1) << 3))) << 4);
#pragma unroll
    for (int n = 0; n < 4; ++n) { VMCNT(3); dma_nt(ru, vt ^ ((n >> 1) << 6), (cq * 128 + 16 * w + 4 * n) * 8192 + hu * 256, 32768 + (4 * w + n) * 1024); }
    VMCNT(4);
    bar();
    STAMP(1);
    if (w >= 4) {
      const unsigned tag = __builtin_amdgcn_readfirstlane(*LP(unsigned, B_EP)) + 1u;
      f32x4 d = {0.f, 0.f, 0.f, 0.f};
      gemv_n<1>(&d, w - 4, 0, B_X, c, g);
      if (c < 4) st_sc(v4u{fu(c < 2 ? d[0] : d[2]), tag, fu(c < 2 ? d[1] : d[3]), tag}, ws, (c & 1) * 1024 + g * 32 + (c >> 1) * 16, WS_QA + hq * 4096 + type * 2048 + (col & 1) * 512 + (w - 4) * 128);
      VMCNT(1);
    } else {
      VMCNT(0);
    }
    bar();
    STAMP(12);
    if (w > 0) { lds_wait(B_CNT + 20, 1u); STAMP(9); }
  }
  if (w > 0) {
    b_stream<3, 7168, W1>(rk, rv, ws, pbase + 21 * sp * 8192 + (w - 1) * 1024, w, lane, tq ? 7 : 8, obase, mlbase ST_PASS);
    STAMP_FLUSH();
    return;
  }
  if (!tq) {
    lds_wait(B_CNT + 12, 7u);
    STAMP(1);
    const unsigned tag = __builtin_amdgcn_readfirstlane(*LP(unsigned, B_EP)) + 1u;
    f32x4 d[4];
#pragma unroll
    for (int ct = 0; ct < 4; ++ct) d[ct] = f32x4{0.f, 0.f, 0.f, 0.f};
#pragma unroll 1
    for (int part = 0; part < 2; ++part) {
      if (part) { VMCNT(0); lds_wait(B_CNT + 16, 7u); STAMP(2); }
      gemv_n<4>(d, 0, part * 32768, B_X + part * 1024, c, g);
    }
    const int ct = (c >> 1) & 3;
    const f32x4 dd = ct == 0 ? d[0] : ct == 1 ? d[1] : ct == 2 ? d[2] : d[3];
    if (c < 8) st_sc(v4u{pk(dd[0], dd[1]), tag, pk(dd[2], dd[3]), tag}, ws, (c & 1) * 16384 + ct * 64 + g * 16, WS_QB + col * 256);
    STAMP(11);
    fetch_qb(ws, WS_QB + (b * 8 + x) * 2048, tag, lane);
    STAMP(13);
    b_stream<1, 1024, WCTL>(rk, rv, ws, pbase + (252 + (k3 & 3)) * 8192, 0, lane, 8, obase, mlbase ST_PASS);
    lds_wait(B_CNT + 4, 8u);
    STAMP(9);
    const int slice = 8 * x + k3;
    final_reduce<4>(ws, WS_OPB + slice * 4096, slice, p.out, 512, tag, lane);
    STAMP(10);
    if (slice == 0) {
      for (unsigned spins = 0;; ++spins) {
        const v4u f = ld_sc(ws, lane * 512, WS_AML);
        if (all_ok(tags_ok(f, tag)) || spins >= SPIN_CAP) break;
        __builtin_amdgcn_s_sleep(1);
      }
      if (lane == 0) __builtin_amdgcn_raw_buffer_store_b32(tag, ws, 0, WS_EPOCH, AUX_SC1);
    }
    STAMP_FLUSH();
    return;
  }
  const unsigned tag = __builtin_amdgcn_readfirstlane(*LP(unsigned, B_EP)) + 1u;
  {
    v4u wf[32];
    {
      const int r0 = 32768 + (8 * (c >> 2) + (c & 3)) * 256;
#pragma unroll
      for (int s = 0; s < 4; ++s) {
        int a = r0 + (((4 * s + g) ^ c) << 4);
        PIN(a);
#pragma unroll
        for (int u = 0; u < 8; ++u) wf[4 * u + s] = lds128(a + (u >> 1) * 8192 + (u & 1) * 1024);
      }
    }
    v4u f[4];
    for (unsigned spins = 0;; ++spins) {
#pragma unroll
      for (int n = 0; n < 4; ++n) f[n] = ld_sc(ws, lane * 16 + n * 1024, WS_QA + hu * 4096);
      if (all_ok(tags_ok(f[0], tag) && tags_ok(f[1], tag) && tags_ok(f[2], tag) && tags_ok(f[3], tag)) || spins >= SPIN_CAP) break;
      asm volatile("" ::: "memory");
    }
    STAMP(2);
    *LP(unsigned, B_QA + lane * 4) = pk(uf(f[0][0]) + uf(f[2][0]), uf(f[0][2]) + uf(f[2][2]));
    *LP(unsigned, B_QA + 256 + lane * 4) = pk(uf(f[1][0]) + uf(f[3][0]), uf(f[1][2]) + uf(f[3][2]));
    v4u qf[4];
#pragma unroll
    for (int s = 0; s < 4; ++s) qf[s] = lds128(B_QA + (c & 1) * 256 + 64 * s + 16 * g);
    f32x4 e[8];
#pragma unroll
    for (int u = 0; u < 8; ++u) e[u] = f32x4{0.f, 0.f, 0.f, 0.f};
#pragma unroll
    for (int s = 0; s < 4; ++s)
#pragma unroll
      for (int u = 0; u < 8; ++u) e[u] = mfma16(wf[4 * u + s], qf[s], e[u]);
    const int ct = (c >> 1) & 3;
    const f32x4 d0 = ct == 0 ? e[0] : ct == 1 ? e[2] : ct == 2 ? e[4] : e[6], d1 = ct == 0 ? e[1] : ct == 1 ? e[3] : ct == 2 ? e[5] : e[7];
    if (c < 8) st_sc(v4u{pk(d0[0], d0[1]), pk(d0[2], d0[3]), pk(d1[0], d1[1]), pk(d1[2], d1[3])}, ws, (c & 1) * 32768 + ct * 64 + g * 16, WS_QABS + hu * 1024 + cq * 256);
    STAMP(3);
    VMCNT(0);
    if (lane == 0) __builtin_amdgcn_raw_buffer_store_b64(v2u{tag, ~tag}, ws, WS_QFLAG + hu * FSTR + cq * 8, 0, AUX_SC1);
    STAMP(4);
    if (lane == 0) *(volatile LAS unsigned*)(size_t)(B_CNT + 20) = 1u;
  }
  for (int k = 0; k < QBDELAY; ++k) __builtin_amdgcn_s_sleep(8);
  STAMP(5);
  fetch_qb(ws, WS_QB + (b * 8 + x) * 2048, tag, lane);
  STAMP(6);
  {
    const int k2 = 2 * k3 + type, h = 4 * x + (k2 >> 2), cg = k2 & 3;
    const rsrc_t ro = mk(p.wob, 4096 * 1024);
    v4u wo[32];
#pragma unroll
    for (int n = 0; n < 32; ++n) { wo[n] = ld_nt(ro, g * 8192 + c * 16, (h * 128 + 32 * (n >> 3) + (n & 7)) * 1024 + cg * 256); vmcnt<WCTL - 1>(); SB(); }
#pragma unroll
    for (int t = 0; t < 4; ++t) transpose8(wo + 8 * t);
#pragma unroll
    for (int n = 0; n < 32; ++n) PIN(wo[n]);
    STAMP(7);
    lds_wait(B_CNT, 7u);
    float wt[12];
    {
      const bool act = (lane & 31) < 12;
      v4u f;
      for (unsigned spins = 0;; ++spins) {
        f = ld_sc(ws, (lane >> 5) * 2048 + (act ? (lane & 31) : 0) * 128, WS_BML + x * 4096 + (h & 3) * 16);
        if (all_ok(tags_ok(f, tag)) || spins >= SPIN_CAP) break;
        __builtin_amdgcn_s_sleep(4);
      }
      const float mm = uf(f[0]), M = row_max(mm);
      const float e = act ? ex2((mm - M) * C2) : 0.f, Lt = row_sum(e * uf(f[2]));
      if ((lane & 16) == 0) *LP(float, B_WT + ((lane >> 5) * 16 + c) * 4) = e / Lt;
      asm volatile("" ::: "memory");
#pragma unroll
      for (int s = 0; s < 3; ++s) {
        const f32x4 t = *LP(f32x4, B_WT + (lane >> 5) * 64 + s * 16);
        wt[4 * s] = t[0]; wt[4 * s + 1] = t[1]; wt[4 * s + 2] = t[2]; wt[4 * s + 3] = t[3];
      }
    }
    STAMP(8);
    lds_wait(B_CNT + 4, 7u);
    float o[4];
    for (unsigned spins = 0;; ++spins) {
      v4u v[12];
#pragma unroll
      for (int s = 0; s < 12; ++s) v[s] = ld_sc(ws, lane * 16 + (s & 3) * 1024, WS_BO + h * 12288 + (s >> 2) * 4096);
      bool ok = true;
      o[0] = o[1] = o[2] = o[3] = 0.f;
#pragma unroll
      for (int s = 0; s < 12; ++s) {
        ok = ok && tags_ok(v[s], tag);
        o[0] = fmaf(wt[s], bflo(v[s][0]), o[0]); o[1] = fmaf(wt[s], bfhi(v[s][0]), o[1]);
        o[2] = fmaf(wt[s], bflo(v[s][2]), o[2]); o[3] = fmaf(wt[s], bfhi(v[s][2]), o[3]);
      }
      if (all_ok(ok) || spins >= SPIN_CAP) break;
      asm volatile("" ::: "memory");
    }
    STAMP(9);
    *LP(v2u, B_QA + lane * 8) = v2u{pk(o[0], o[1]), pk(o[2], o[3])};
    asm volatile("" ::: "memory");
    out_gemv<4>(wo, B_QA, ws, WS_OPB + cg * 16 * 4096 + h * 128, 4096, tag, lane);
    STAMP(10);
  }
  STAMP_FLUSH();
}

__global__ void __launch_bounds__(512, 2) k_fused(Params p) {
  const int lane = threadIdx.x & 63, w = __builtin_amdgcn_readfirstlane(threadIdx.x >> 6);
  const int x = blockIdx.x & 7, i = blockIdx.x >> 3;
  if (w >= 6 && i + 32 * (7 - w) < CODE_KB) {
    const rsrc_t rcode = mk((const void*)(__builtin_amdgcn_s_getpc() & ~255ull), CODE_KB * 1024);
    dma_df(rcode, lane * 16, (i + 32 * (7 - w)) * 1024, i >= 24 ? 2048 : 71680);
  }
  if (i >= 24) role_a(p, x, i - 24, lane, w);
  else role_b(p, x, i, lane, w);
}

__global__ void k_amp(const unsigned* st, int mult) {
  const unsigned ref = st[0];
  int mn = 0x7fffffff, mx = -0x7fffffff;
  for (int i = threadIdx.x; i < 2048 * 16; i += 256) { const int b = i >> 7, w = (i >> 4) & 7, k = i & 15; const unsigned v = st[i]; if (!v) continue; const int d = (int)(v - ref);
    if (k == 0) mn = d < mn ? d : mn;
    if (k < 15) mx = d > mx ? d : mx; }
  __shared__ int a[256], c[256];
  a[threadIdx.x] = mn; c[threadIdx.x] = mx; __syncthreads();
  if (threadIdx.x == 0) { for (int i = 0; i < 256; ++i) { mn = a[i] < mn ? a[i] : mn; const int d = c[i]; mx = d > mx ? d : mx; }
    unsigned long long dd = (unsigned long long)(mx - mn) * mult; if (dd > 200000) dd = 200000;
    unsigned long long t0 = __builtin_amdgcn_s_memrealtime();
    while (__builtin_amdgcn_s_memrealtime() - t0 < dd) __builtin_amdgcn_s_sleep(1); }
}
extern "C" void kernel_launch(void* const* d_in, const int* in_sizes, int n_in, void* d_out, int out_size, void* d_ws, size_t ws_size, hipStream_t stream) {
  Params p;
  p.xa = d_in[0]; p.ckv = d_in[1]; p.wqa = d_in[2]; p.wuk = d_in[3]; p.wuv = d_in[4]; p.woa = d_in[5];
  p.xb = d_in[6]; p.kc = d_in[7]; p.vc = d_in[8]; p.wqb = d_in[9]; p.wob = d_in[10];
  p.out = d_out; p.ws = d_ws;
  (void)hipFuncSetAttribute((const void*)k_fused, hipFuncAttributeMaxDynamicSharedMemorySize, 163840);
  k_fused<<<256, 512, 163840, stream>>>(p);
  k_amp<<<1, 256, 0, stream>>>((const unsigned*)((char*)d_ws + WS_ST), 0);
}
```

```cpp
#define STAMPS 1
#include <hip/hip_runtime.h>
#include <hip/hip_bf16.h>

#define LAS __attribute__((address_space(3)))
#define DI __device__ __forceinline__
typedef unsigned v4u __attribute__((ext_vector_type(4)));
typedef unsigned v2u __attribute__((ext_vector_type(2)));
typedef float f32x4 __attribute__((ext_vector_type(4)));
typedef float f32x2 __attribute__((ext_vector_type(2)));
typedef float f32x16 __attribute__((ext_vector_type(16)));
typedef __bf16 bf16x8 __attribute__((ext_vector_type(8)));
typedef __bf16 bf16x2 __attribute__((ext_vector_type(2)));
typedef short v4s __attribute__((ext_vector_type(4)));
typedef unsigned short u16;
typedef __amdgpu_buffer_rsrc_t rsrc_t;
typedef unsigned lds_t;
#define LP(T, a) ((LAS T*)(size_t)(unsigned)(a))

constexpr int AUX_NT = 2, AUX_SC1 = 16;
constexpr float C2 = 0.088388347648318440f * 1.4426950408889634f;
constexpr unsigned SPIN_CAP = 1u << 16, LSPIN = 1u << 20;

constexpr int WS_EPOCH = 0;
constexpr int WS_QFLAG = 16384;
constexpr int WS_QA = 1572864;
constexpr int WS_QB = 65536;
constexpr int WS_AML = 98304;
constexpr int WS_QABS = 131072;
constexpr int WS_BML = 196608;
constexpr int WS_OPB = 262144;
constexpr int WS_OPA = 524288;
constexpr int WS_BO = 1048576;
constexpr int WS_AO = 2097152;
constexpr int WS_BYTES = 4194304;
constexpr int WS_ST = 4194304;

struct Params {
  const void *xa, *ckv, *wqa, *wuk, *wuv, *woa, *xb, *kc, *vc, *wqb, *wob;
  void *out, *ws;
};

DI rsrc_t mk(const void* p, int bytes) { return __builtin_amdgcn_make_buffer_rsrc((void*)p, (short)0, bytes, 0x00020000); }
DI v4u ld_nt(rsrc_t r, int voff, int soff) { return __builtin_amdgcn_raw_buffer_load_b128(r, voff, soff, AUX_NT); }
DI v4u ld_sc(rsrc_t r, int voff, int soff) { return __builtin_amdgcn_raw_buffer_load_b128(r, voff, soff, AUX_SC1); }
DI void st_sc(v4u v, rsrc_t r, int voff, int soff) { __builtin_amdgcn_raw_buffer_store_b128(v, r, voff + soff, 0, AUX_SC1); }
DI void dma_nt(rsrc_t r, int voff, int soff, unsigned lds) {
  asm volatile("s_mov_b32 m0, %0\n\ts_nop 0\n\tbuffer_load_dwordx4 %1, %2, %3 offen nt lds" ::"s"(lds), "v"(voff), "s"(r), "s"(soff) : "memory");
}
DI void dma_df(rsrc_t r, int voff, int soff, unsigned lds) {
  asm volatile("s_mov_b32 m0, %0\n\ts_nop 0\n\tbuffer_load_dwordx4 %1, %2, %3 offen lds" ::"s"(lds), "v"(voff), "s"(r), "s"(soff) : "memory");
}
DI void dma_sc(rsrc_t r, int voff, int soff, unsigned lds) {
  asm volatile("s_mov_b32 m0, %0\n\ts_nop 0\n\tbuffer_load_dwordx4 %1, %2, %3 offen sc1 lds" ::"s"(lds), "v"(voff), "s"(r), "s"(soff) : "memory");
}
#define VMCNT(n) asm volatile("s_waitcnt vmcnt(" #n ")" ::: "memory")
template <int N> DI void vmcnt() { asm volatile("s_waitcnt vmcnt(%0)" ::"i"(N) : "memory"); }
#define SB() __builtin_amdgcn_sched_barrier(0)
#define PIN(x) asm volatile("" : "+v"(x))
#define W1 6
#define PFN 10
#define PFWV 1
#define WQK 8
#define WCTL 5
#define WBETA 7
#define WO_EARLY 8
#define WV_S 5
#define FSTR 32
#define QDELAY 0
#define ILV 1
#define QBDELAY 0
#define HOLD 0
#define CODE_KB 38
DI void bar() { asm volatile("s_waitcnt lgkmcnt(0)\n\ts_barrier" ::: "memory"); }
DI float bflo(unsigned u) { return __builtin_bit_cast(float, u << 16); }
DI float bfhi(unsigned u) { return __builtin_bit_cast(float, u & 0xffff0000u); }
DI unsigned fu(float f) { return __builtin_bit_cast(unsigned, f); }
DI float uf(unsigned u) { return __builtin_bit_cast(float, u); }
DI unsigned pk(float lo, float hi) { f32x2 v = {lo, hi}; return __builtin_bit_cast(unsigned, __builtin_convertvector(v, bf16x2)); }
DI float ex2(float x) { return __builtin_amdgcn_exp2f(x); }
DI f32x4 mfma16(v4u a, v4u b, f32x4 c) { return __builtin_amdgcn_mfma_f32_16x16x32_bf16(__builtin_bit_cast(bf16x8, a), __builtin_bit_cast(bf16x8, b), c, 0, 0, 0); }
DI f32x16 mfma32(v4u a, v4u b, f32x16 c) { return __builtin_amdgcn_mfma_f32_32x32x16_bf16(__builtin_bit_cast(bf16x8, a), __builtin_bit_cast(bf16x8, b), c, 0, 0, 0); }
DI v4u lds128(lds_t p) { return *LP(v4u, p); }
DI v2u ldstr(lds_t p) { return __builtin_bit_cast(v2u, __builtin_amdgcn_ds_read_tr16_b64_v4i16(LP(v4s, p))); }
DI float x16_max(float x) { auto s = __builtin_amdgcn_permlane16_swap(fu(x), fu(x), false, false); return fmaxf(uf(s[0]), uf(s[1])); }
DI float x32_max(float x) { auto s = __builtin_amdgcn_permlane32_swap(fu(x), fu(x), false, false); return fmaxf(uf(s[0]), uf(s[1])); }
DI float x16_sum(float x) { auto s = __builtin_amdgcn_permlane16_swap(fu(x), fu(x), false, false); return uf(s[0]) + uf(s[1]); }
DI float x32_sum(float x) { auto s = __builtin_amdgcn_permlane32_swap(fu(x), fu(x), false, false); return uf(s[0]) + uf(s[1]); }
template <int CTRL> DI float dpp(float x) { return uf(__builtin_amdgcn_mov_dpp(fu(x), CTRL, 0xf, 0xf, true)); }
constexpr int XOR1 = 0xB1, XOR2 = 0x4E, ROR4 = 0x124, ROR8 = 0x128;
DI float row_max(float x) { x = fmaxf(x, dpp<XOR1>(x)); x = fmaxf(x, dpp<XOR2>(x)); x = fmaxf(x, dpp<ROR4>(x)); return fmaxf(x, dpp<ROR8>(x)); }
DI float row_sum(float x) { x += dpp<XOR1>(x); x += dpp<XOR2>(x); x += dpp<ROR4>(x); return x + dpp<ROR8>(x); }
DI unsigned fsw(unsigned row) { return ((row & 3) << 2) | ((row >> 2) & 3); }
DI bool tags_ok(v4u v, unsigned tag) { return v[1] == tag && v[3] == tag; }
DI bool all_ok(bool ok) { return __builtin_amdgcn_ballot_w64(!ok) == 0ull; }
DI v4u tfrag(const v4u* r, int i) {
  v4u o;
#pragma unroll
  for (int t = 0; t < 4; ++t) o[t] = __builtin_amdgcn_perm(r[2 * t + 1][i >> 1], r[2 * t][i >> 1], (i & 1) ? 0x07060302u : 0x05040100u);
  return o;
}
DI void transpose8(v4u* r) {
  v4u o[8];
#pragma unroll
  for (int i = 0; i < 8; ++i) o[i] = tfrag(r, i);
#pragma unroll
  for (int i = 0; i < 8; ++i) r[i] = o[i];
}

#define STAMP(i) do { SB(); stv[i] = (unsigned)__builtin_amdgcn_s_memrealtime(); SB(); } while (0)
#define STAMP_DECL unsigned stv[16]; _Pragma("unroll") for (int i_ = 0; i_ < 16; ++i_) stv[i_] = 0; const unsigned clk0_ = (unsigned)__builtin_amdgcn_s_memtime();
#define STAMP_FLUSH() do { { const unsigned c1_ = (unsigned)__builtin_amdgcn_s_memtime(), r1_ = (unsigned)__builtin_amdgcn_s_memrealtime(); stv[15] = stv[0] + (c1_ - clk0_) * 10u / (r1_ - stv[0]); } if (lane == 0) { _Pragma("unroll") for (int i_ = 0; i_ < 16; ++i_) ((unsigned*)((char*)p.ws + WS_ST))[(blockIdx.x * 8 + w) * 16 + i_] = stv[i_]; } } while (0)

template <int KS> DI void out_gemv(const v4u* wf, lds_t ystage, rsrc_t ws, int dst, int slice_stride, unsigned tag, int lane) {
  const int c = lane & 15, g = lane >> 4;
  v4u a[KS];
#pragma unroll
  for (int t = 0; t < KS; ++t) a[t] = lds128(ystage + ((c >> 2) & 1) * (64 * KS) + 64 * t + 16 * g);
  f32x4 d[8];
#pragma unroll
  for (int i = 0; i < 8; ++i) d[i] = f32x4{0.f, 0.f, 0.f, 0.f};
#pragma unroll
  for (int t = 0; t < KS; ++t)
#pragma unroll
    for (int i = 0; i < 8; ++i) d[i] = mfma16(a[t], wf[8 * t + i], d[i]);
  const bool hi = lane >= 32;
  float v[4];
#pragma unroll
  for (int i = 0; i < 4; ++i) v[i] = hi ? d[4 + i][0] : d[i][0];
  const int voff = (lane >> 3) * slice_stride + (lane & 7) * 16;
  st_sc(v4u{fu(v[0]), tag, fu(v[1]), tag}, ws, voff, dst);
  st_sc(v4u{fu(v[2]), tag, fu(v[3]), tag}, ws, voff, dst + 8 * slice_stride);
}
template <int NL> DI void final_reduce(rsrc_t ws, int src, int slice, void* out, int colbase, unsigned tag, int lane) {
  float s0, s1;
  for (unsigned spins = 0;; ++spins) {
    v4u v[NL];
#pragma unroll
    for (int i = 0; i < NL; ++i) v[i] = ld_sc(ws, lane * 16 + i * 1024, src);
    bool ok = true;
    s0 = 0.f; s1 = 0.f;
#pragma unroll
    for (int i = 0; i < NL; ++i) { ok = ok && tags_ok(v[i], tag); s0 += uf(v[i][0]); s1 += uf(v[i][2]); }
    if (all_ok(ok) || spins >= SPIN_CAP) break;
    asm volatile("" ::: "memory");
  }
  s0 += dpp<ROR8>(s0); s1 += dpp<ROR8>(s1);
  s0 = x32_sum(x16_sum(s0)); s1 = x32_sum(x16_sum(s1));
  if (lane < 8) {
    const int l = 8 * (slice & 7) + lane, c = l & 15, g = l >> 4;
    const int col = 128 * (slice >> 4) + 8 * c + 4 * (g >> 1) + 2 * ((slice >> 3) & 1);
    *(unsigned*)((u16*)out + (g & 1) * 1024 + colbase + col) = pk(s0, s1);
  }
}

DI void role_a(const Params& p, int x, int i, int lane, int w) {
  STAMP_DECL
  STAMP(0);
  const int b = x & 1, sp = (x >> 1) * 8 + i, h = 4 * ((i & 6) + (x & 1)) + (((x >> 1) + (i >> 1)) & 3), dh = i & 1;
  const int c = lane & 15, g = lane >> 4;
  const rsrc_t ws = mk(p.ws, WS_BYTES), rc = mk(p.ckv, 2 * 8192 * 1024);
  constexpr int IMG = 32768;
  const int kbase = (b * 8192 + sp * 256) * 1024;
  auto krow = [&](int row) { return kbase + ((row + 32 * i) & 255) * 1024; };
  if (w == 0) dma_sc(ws, 0, WS_EPOCH, 1024);
  {
    const int v0 = (lane ^ fsw(2 * w)) << 4, v1 = (lane ^ fsw(2 * w + 1)) << 4;
#pragma unroll
    for (int j = 0; j < 16; ++j) { const int row = 2 * w + (j & 1) + 16 * (j >> 1); dma_nt(rc, (j & 1) ? v1 : v0, krow(row), IMG + row * 1024); }
  }
  int ts = c * 1024 + 256 * g + fsw(c) * 16;
  const int hh = lane >> 5, blk = (lane >> 4) & 1, q4 = (lane & 15) >> 2, p4 = lane & 3;
  int ta[2][2];
#pragma unroll
  for (int ct = 0; ct < 2; ++ct)
#pragma unroll
    for (int t = 0; t < 2; ++t) { const int row = 8 * hh + 4 * t + q4; ta[ct][t] = IMG + row * 1024 + (((8 * w + 4 * ct + 2 * blk + (p4 >> 1)) ^ fsw(row)) << 4) + 8 * (p4 & 1); PIN(ta[ct][t]); }
  auto trfrag = [&](int ct, int ks) { const v2u r0 = ldstr(ta[ct][0] + ks * 16384), r1 = ldstr(ta[ct][1] + ks * 16384); return v4u{r0[0], r0[1], r1[0], r1[1]}; };
  int vv[4];
#pragma unroll
  for (int j = 0; j < 4; ++j) vv[j] = (lane ^ fsw(4 * (w & 3) + j)) << 4;
  v4u sa[16], pa[8][2];
  VMCNT(8);
  bar();
  STAMP(1);
  const unsigned tag = __builtin_amdgcn_readfirstlane(*LP(unsigned, 1024)) + 1u;
  if (w < 4) {
#pragma unroll
    for (int s = 0; s < 16; ++s) sa[s] = lds128(IMG + w * 16384 + (ts ^ (s << 4)));
#pragma unroll
    for (int s = 0; s < 16; ++s) PIN(sa[s]);
  }
#pragma unroll
  for (int ks = 0; ks < 4; ++ks) { pa[ks][0] = trfrag(0, ks); pa[ks][1] = trfrag(1, ks); }
#pragma unroll
  for (int ks = 0; ks < 4; ++ks) { PIN(pa[ks][0]); PIN(pa[ks][1]); }
  bar();
  if (w >= 4) {
#pragma unroll
    for (int j = 0; j < 16; ++j) { const int row = 4 * (w - 4) + (j & 3) + 16 * (j >> 2); dma_nt(rc, vv[j & 3], krow(128 + row), IMG + row * 1024); }
    VMCNT(16);
  } else {
    VMCNT(0);
  }
  bar();
  STAMP(2);
  PIN(ts);
  if (w >= 4) {
#pragma unroll
    for (int s = 0; s < 16; ++s) sa[s] = lds128(IMG + w * 16384 + (ts ^ (s << 4)));
#pragma unroll
    for (int s = 0; s < 16; ++s) PIN(sa[s]);
  }
#pragma unroll
  for (int ks = 4; ks < 8; ++ks) { pa[ks][0] = trfrag(0, ks); pa[ks][1] = trfrag(1, ks); }
#pragma unroll
  for (int ks = 4; ks < 8; ++ks) { PIN(pa[ks][0]); PIN(pa[ks][1]); }
  bar();
  STAMP(13);
  if (w >= 4) {
#pragma unroll
    for (int j = 16; j < 32; ++j) { const int row = 4 * (w - 4) + (j & 3) + 16 * (j >> 2); vmcnt<WBETA - 1>(); dma_nt(rc, vv[j & 3], krow(128 + row), IMG + row * 1024); }
    STAMP(3);
  } else if (w == 0) {
    for (int k = 0; k < QDELAY; ++k) __builtin_amdgcn_s_sleep(8);
    for (unsigned spins = 0;; ++spins) {
      const v4u f = ld_sc(ws, (lane >> 1) * FSTR + (lane & 1) * 16, WS_QFLAG);
      if (all_ok(f[0] == tag && f[1] == ~tag && f[2] == tag && f[3] == ~tag) || spins >= SPIN_CAP) break;
      __builtin_amdgcn_s_sleep(2);
    }
    STAMP(3);
#pragma unroll
    for (int j = 0; j < 32; ++j) dma_sc(ws, (lane ^ fsw(j)) << 4, WS_QABS + b * 32768 + j * 1024, j * 1024);
  }
  VMCNT(0);
  bar();
  STAMP(4);
  PIN(ts);
  f32x4 d[2][2];
#pragma unroll
  for (int k = 0; k < 4; ++k) d[k >> 1][k & 1] = f32x4{0.f, 0.f, 0.f, 0.f};
  v4u wv[8], wo[16];
  const rsrc_t rv = mk(p.wuv, 512 * 8192);
  const int vo = (lane >> 3) * 8 * 8192 + (lane & 7) * 16;
  const rsrc_t ro = mk(p.woa, 4096 * 1024);
  auto ld_wo = [&](int n) { return ld_nt(ro, g * 8192 + c * 16, (h * 128 + dh * 64 + 32 * (n >> 3) + (n & 7)) * 1024 + w * 256); };
  {
    v4u q0[3], q1[3], sb[3];
#pragma unroll
    for (int s = 0; s < 18; ++s) {
      if (s < 16) { const int a = ts ^ (s << 4); q0[s % 3] = lds128(a); q1[s % 3] = lds128(a + 16384); sb[s % 3] = lds128(a + (IMG + w * 16384)); }
      SB();
      if (s >= 2) {
        const int t = s - 2;
        d[0][0] = mfma16(sa[t], q0[t % 3], d[0][0]); d[0][1] = mfma16(sa[t], q1[t % 3], d[0][1]);
        d[1][0] = mfma16(sb[t % 3], q0[t % 3], d[1][0]); d[1][1] = mfma16(sb[t % 3], q1[t % 3], d[1][1]);
        { const int k = t < 3 ? t : (t % 3 == 2 ? 2 + t / 3 : -1);
          if (k >= 0) wv[k] = ld_nt(rv, vo, (64 * w + k) * 8192 + h * 256 + dh * 128); }
      }
      SB();
    }
  }
  float m[2];
#pragma unroll
  for (int hg = 0; hg < 2; ++hg) {
    float t = fmaxf(fmaxf(fmaxf(d[0][hg][0], d[0][hg][1]), fmaxf(d[0][hg][2], d[0][hg][3])), fmaxf(fmaxf(d[1][hg][0], d[1][hg][1]), fmaxf(d[1][hg][2], d[1][hg][3])));
    m[hg] = x32_max(x16_max(t));
  }
  constexpr int PST = 528, MX = 17408, LS = 18432, WT = 19456;
  bar();
  STAMP(5);
  if (g < 2) *LP(float, MX + ((16 * g + c) * 8 + w) * 4) = g ? m[1] : m[0];
  bar();
  wv[7] = ld_nt(rv, vo, (64 * w + 7) * 8192 + h * 256 + dh * 128);
#pragma unroll
  for (int hg = 0; hg < 2; ++hg) {
    const f32x4 a0 = *LP(f32x4, MX + (16 * hg + c) * 32), a1 = *LP(f32x4, MX + (16 * hg + c) * 32 + 16);
    m[hg] = fmaxf(fmaxf(fmaxf(a0[0], a0[1]), fmaxf(a0[2], a0[3])), fmaxf(fmaxf(a1[0], a1[1]), fmaxf(a1[2], a1[3])));
  }
  float l[2];
#pragma unroll
  for (int hg = 0; hg < 2; ++hg) {
    const float mc = -m[hg] * C2;
    float s = 0.f;
#pragma unroll
    for (int ab = 0; ab < 2; ++ab) {
      float e[4];
#pragma unroll
      for (int r = 0; r < 4; ++r) { e[r] = ex2(fmaf(d[ab][hg][r], C2, mc)); s += e[r]; }
      *LP(v2u, (16 * hg + c) * PST + (128 * ab + 16 * w + 4 * g) * 2) = v2u{pk(e[0], e[1]), pk(e[2], e[3])};
    }
    l[hg] = x32_sum(x16_sum(s));
  }
  if (g < 2) *LP(float, LS + ((16 * g + c) * 8 + w) * 4) = g ? l[1] : l[0];
  const float Mh = (g & 1) ? m[1] : m[0];
  bar();
  STAMP(6);
  f32x16 o[2];
#pragma unroll
  for (int k = 0; k < 16; ++k) { o[0][k] = 0.f; o[1][k] = 0.f; }
  {
    int pb = (lane & 31) * PST + 16 * hh;
    PIN(pb); PIN(ta[0][0]); PIN(ta[0][1]); PIN(ta[1][0]); PIN(ta[1][1]);
    v4u pf[3], tb[3][2];
#pragma unroll
    for (int s = 0; s < 18; ++s) {
      if (s < 16) { pf[s % 3] = lds128(pb + 32 * s); if (s >= 8) { tb[s % 3][0] = trfrag(0, s - 8); tb[s % 3][1] = trfrag(1, s - 8); } }
      SB();
      if (s >= 2) {
        const int t = s - 2;
        o[0] = mfma32(t < 8 ? pa[t][0] : tb[t % 3][0], pf[t % 3], o[0]);
        o[1] = mfma32(t < 8 ? pa[t][1] : tb[t % 3][1], pf[t % 3], o[1]);
        if (w < 4 && !(t & 1) && (t >> 1) < WO_EARLY) wo[t >> 1] = ld_wo(t >> 1);
      }
      SB();
    }
  }
  float Lh;
  { const f32x4 a0 = *LP(f32x4, LS + (lane & 31) * 32), a1 = *LP(f32x4, LS + (lane & 31) * 32 + 16); Lh = ((a0[0] + a0[1]) + (a0[2] + a0[3])) + ((a1[0] + a1[1]) + (a1[2] + a1[3])); }
  bar();
  STAMP(7);
  {
    const lds_t stg = IMG + w * 4608;
#pragma unroll
    for (int ct = 0; ct < 2; ++ct)
#pragma unroll
      for (int rq = 0; rq < 4; ++rq)
        *LP(v2u, stg + (lane & 31) * 144 + (32 * ct + 8 * rq + 4 * hh) * 2) = v2u{pk(o[ct][4 * rq], o[ct][4 * rq + 1]), pk(o[ct][4 * rq + 2], o[ct][4 * rq + 3])};
#pragma unroll
    for (int e = 0; e < 4; ++e) {
      const v4u v = lds128(stg + (8 * e + (lane >> 3)) * 144 + (lane & 7) * 16);
      st_sc(v, ws, (lane >> 3) * 65536 + (lane & 7) * 16, WS_AO + e * 8 * 65536 + (b * 32 + sp) * 1024 + w * 128);
    }
  }
  VMCNT(0);
  bar();
  STAMP(8);
  if (w == 7 && lane < 32) st_sc(v4u{fu(Mh), tag, fu(Lh), tag}, ws, lane * 16, WS_AML + (b * 32 + sp) * 512);
  if (w < 4) {
#pragma unroll
    for (int n = WO_EARLY; n < 16; ++n) wo[n] = ld_wo(n);
  }
  SB();
  transpose8(wv);
#pragma unroll
  for (int n = 0; n < 8; ++n) PIN(wv[n]);
  constexpr int XS = IMG + 40960, G1 = XS + 8448, YS = G1 + 8192;
  {
    v4u f;
    for (unsigned spins = 0;; ++spins) {
      f = ld_sc(ws, (lane & 7) * 512, WS_AML + w * 4096 + h * 16);
      if (all_ok(tags_ok(f, tag)) || spins >= SPIN_CAP) break;
      __builtin_amdgcn_s_sleep(2);
    }
    asm volatile("" ::: "memory");
    STAMP(9);
    v4u t[8];
#pragma unroll
    for (int k = 0; k < 8; ++k) t[k] = ld_sc(ws, lane * 16 + (k & 3) * 1024, WS_AO + h * 65536 + w * 8192 + (k >> 2) * 4096);
    SB();
    const float mm = uf(f[0]);
    float M = fmaxf(mm, dpp<XOR1>(mm)); M = fmaxf(M, dpp<XOR2>(M)); M = fmaxf(M, dpp<ROR4>(M));
    const float e = ex2((mm - M) * C2);
    float Lw = e * uf(f[2]); Lw += dpp<XOR1>(Lw); Lw += dpp<XOR2>(Lw); Lw += dpp<ROR4>(Lw);
    if (lane == 0) *LP(f32x2, WT + w * 8) = f32x2{M, Lw};
    if (w < 4) { transpose8(wo); transpose8(wo + 8); }
    float xs[8];
#pragma unroll
    for (int j = 0; j < 8; ++j) xs[j] = 0.f;
#pragma unroll
    for (int k = 0; k < 8; ++k) {
      const float wt = uf(__builtin_amdgcn_readlane(fu(e), k));
#pragma unroll
      for (int j = 0; j < 4; ++j) { xs[2 * j] = fmaf(wt, bflo(t[k][j]), xs[2 * j]); xs[2 * j + 1] = fmaf(wt, bfhi(t[k][j]), xs[2 * j + 1]); }
    }
    *LP(v4u, XS + w * 1056 + lane * 16) = v4u{pk(xs[0], xs[1]), pk(xs[2], xs[3]), pk(xs[4], xs[5]), pk(xs[6], xs[7])};
  }
  bar();
  STAMP(10);
  {
    const v4u xa = lds128(XS + (4 * (c >> 3) + (c & 3)) * 1056 + (8 * w + 2 * g + ((c >> 2) & 1)) * 16);
    const f32x4 m0 = *LP(f32x4, WT + hh * 32), m1 = *LP(f32x4, WT + hh * 32 + 16);
    const float M = fmaxf(fmaxf(m0[0], m0[2]), fmaxf(m1[0], m1[2]));
    f32x4 fs = {ex2((m0[0] - M) * C2), ex2((m0[2] - M) * C2), ex2((m1[0] - M) * C2), ex2((m1[2] - M) * C2)};
    fs *= 1.0f / (fs[0] * m0[1] + fs[1] * m0[3] + fs[2] * m1[1] + fs[3] * m1[3]);
    float y[8];
#pragma unroll
    for (int jj = 0; jj < 8; ++jj) { const f32x4 dd = mfma16(xa, wv[jj], f32x4{0.f, 0.f, 0.f, 0.f}); y[jj] = (fs[0] * dd[0] + fs[1] * dd[1]) + (fs[2] * dd[2] + fs[3] * dd[3]); }
    if (((lane >> 3) & 1) == (g & 1)) {
      const lds_t dst = G1 + ((2 * w + (g & 1)) * 128 + (lane >> 5) * 64 + 8 * (lane & 7)) * 4;
      *LP(f32x4, dst) = f32x4{y[0], y[1], y[2], y[3]};
      *LP(f32x4, dst + 16) = f32x4{y[4], y[5], y[6], y[7]};
    }
  }
  bar();
  STAMP(11);
  if (w < 4) {
    f32x2 s = {0.f, 0.f};
#pragma unroll
    for (int k = 0; k < 16; ++k) s += *LP(f32x2, G1 + k * 512 + lane * 8);
    const lds_t ys = YS + w * 256;
    *LP(unsigned, ys + lane * 4) = pk(s[0], s[1]);
    asm volatile("" ::: "memory");
    out_gemv<2>(wo, ys, ws, WS_OPA + w * 16 * 8192 + (2 * h + dh) * 128, 8192, tag, lane);
  } else if (w == 4) {
    const int slice = x * 8 + i;
    final_reduce<8>(ws, WS_OPA + slice * 8192, slice, p.out, 0, tag, lane);
  }
  STAMP(12);
  STAMP_FLUSH();
}

constexpr int B_X = 65536, B_QB = 67584, B_QA = 68608, B_ML = 69120, B_E = 69376, B_CNT = 69632, B_WT = 69888, B_EP = 70656, B_KS = 73728;
#define ST_ARG , unsigned* stv
#define ST_PASS , stv

DI void k_write(const v4u& kr, int j, lds_t my, int c, int g) { const int key = 4 * j + g; *LP(v4u, my + key * 256 + ((c ^ (key & 15)) << 4)) = kr; }
DI void k_read(v4u* kr, lds_t my, int c, int g) {
  const int k0 = 4 * (c & 3) + (c >> 2);
#pragma unroll
  for (int T = 0; T < 2; ++T)
#pragma unroll
    for (int s = 0; s < 4; ++s) kr[4 * T + s] = lds128(my + (16 * T + k0) * 256 + (((4 * s + g) ^ k0) << 4));
}
DI unsigned lds_arrive(lds_t cnt, int lane) {
  unsigned old = 0;
  asm volatile("s_waitcnt lgkmcnt(0)" ::: "memory");
  if (lane == 0) old = __hip_atomic_fetch_add(LP(unsigned, cnt), 1u, __ATOMIC_RELAXED, __HIP_MEMORY_SCOPE_WORKGROUP);
  old = __builtin_amdgcn_readfirstlane(old);
  asm volatile("" ::: "memory");
  return old;
}
template <int NU> DI void b_scores(const v4u* kr, v4u* pf, rsrc_t ws, int w, int lane, int nwaves, int mlbase, unsigned tag) {
  const int c = lane & 15, g = lane >> 4;
  v4u qf[4];
#pragma unroll
  for (int s = 0; s < 4; ++s) qf[s] = lds128(B_QB + (c >> 2) * 256 + 64 * s + 16 * g);
  float sc[8 * NU];
#pragma unroll
  for (int T = 0; T < 2 * NU; ++T) {
    f32x4 d = {0.f, 0.f, 0.f, 0.f};
#pragma unroll
    for (int s = 0; s < 4; ++s) d = mfma16(kr[4 * T + s], qf[s], d);
#pragma unroll
    for (int r = 0; r < 4; ++r) sc[4 * T + r] = d[r];
  }
  float m = sc[0];
#pragma unroll
  for (int k = 1; k < 8 * NU; ++k) m = fmaxf(m, sc[k]);
  m = x32_max(x16_max(m));
  float l = 0.f;
  const float mc = -m * C2;
#pragma unroll
  for (int k = 0; k < 8 * NU; ++k) { sc[k] = ex2(fmaf(sc[k], C2, mc)); l += sc[k]; }
  l = x32_sum(x16_sum(l));
#pragma unroll
  for (int u = 0; u < NU; ++u)
#pragma unroll
    for (int t = 0; t < 4; ++t) pf[u][t] = pk(sc[8 * u + 2 * t], sc[8 * u + 2 * t + 1]);
  if (lane < 16 && (c & 3) == 0) *LP(f32x2, B_ML + (w * 4 + (c >> 2)) * 8) = f32x2{m, l};
  if ((int)lds_arrive(B_CNT, lane) == nwaves - 1) {
    const f32x2 ml = *LP(f32x2, B_ML + (lane & 31) * 8);
    float M = ml[0];
    M = fmaxf(M, dpp<ROR4>(M)); M = fmaxf(M, dpp<ROR8>(M)); M = x16_max(M);
    const float e = ex2((ml[0] - M) * C2);
    float t = ml[1] * e;
    t += dpp<ROR4>(t); t += dpp<ROR8>(t); t = x16_sum(t);
    if (lane < 32) *LP(float, B_E + ((lane & 3) * 8 + (lane >> 2)) * 4) = e;
    if (lane < 8) st_sc(v4u{fu(M), tag, fu(t), tag}, ws, lane * 16, mlbase);
  }
}
DI void b_pv(const v4u& pf, const v4u* vr, f32x4* acc) {
  v4u t[8];
#pragma unroll
  for (int j = 0; j < 4; ++j) {
#pragma unroll
    for (int k = 0; k < 8; ++k) t[k][j] = __builtin_amdgcn_perm(vr[2 * j + 1][k >> 1], vr[2 * j][k >> 1], (k & 1) ? 0x07060302u : 0x05040100u);
    SB();
  }
#pragma unroll
  for (int k = 0; k < 8; ++k) acc[k] = mfma16(pf, t[k], acc[k]);
#pragma unroll
  for (int k = 0; k < 8; ++k) PIN(acc[k]);
}
DI void b_merge(const f32x4* acc, rsrc_t ws, int w, int lane, int nwaves, int obase, unsigned tag ST_ARG) {
  const lds_t mo = B_KS + w * 8192 + lane * 32;
  *LP(f32x4, mo) = f32x4{acc[0][0], acc[1][0], acc[2][0], acc[3][0]};
  *LP(f32x4, mo + 16) = f32x4{acc[4][0], acc[5][0], acc[6][0], acc[7][0]};
  if ((int)lds_arrive(B_CNT + 4, lane) == nwaves - 1) {
    int ob = B_KS + lane * 16, eb = B_E + (lane >> 5) * 32;
    PIN(ob); PIN(eb);
    f32x4 e[2][2], v[2][8];
#pragma unroll
    for (int eh = 0; eh < 2; ++eh) {
      e[eh][0] = *LP(f32x4, eb + eh * 64); e[eh][1] = *LP(f32x4, eb + eh * 64 + 16);
#pragma unroll
      for (int k = 0; k < 8; ++k) v[eh][k] = *LP(f32x4, ob + k * 8192 + eh * 1024);
    }
    SB();
#pragma unroll
    for (int eh = 0; eh < 2; ++eh) {
      f32x4 s = e[eh][0][0] * v[eh][0];
#pragma unroll
      for (int k = 1; k < 8; ++k) s += e[eh][k >> 2][k & 3] * v[eh][k];
      st_sc(v4u{pk(s[0], s[1]), tag, pk(s[2], s[3]), tag}, ws, (lane >> 5) * 12288 + (lane & 31) * 16, obase + eh * 2 * 12288);
    }
    STAMP(8);
  }
}
DI void lds_signal(lds_t cnt, int lane) { if (lane == 0) (void)__hip_atomic_fetch_add(LP(unsigned, cnt), 1u, __ATOMIC_RELAXED, __HIP_MEMORY_SCOPE_WORKGROUP); }
DI void lds_wait(lds_t cnt, unsigned want) {
  for (unsigned spins = 0; *(volatile LAS unsigned*)(size_t)cnt < want && spins < LSPIN; ++spins) __builtin_amdgcn_s_sleep(1);
  asm volatile("" ::: "memory");
}
template <int NCT> DI void gemv_n(f32x4* d, int ct0, lds_t wb, lds_t xb, int c, int g) {
  const int q4 = c >> 2, p4 = c & 3, fx = (q4 >> 1) | ((g & 1) << 1);
  int a[NCT];
#pragma unroll
  for (int ct = 0; ct < NCT; ++ct) { a[ct] = wb + (8 * g + q4) * 128 + (((ct0 + ct) ^ fx) << 5) + 8 * p4; PIN(a[ct]); }
  int xa = xb + (c & 1) * 512 + 16 * g;
  PIN(xa);
  constexpr int D = NCT == 1 ? 4 : 1;
  v4u xf[D + 1], wf[D + 1][NCT];
#pragma unroll
  for (int s = 0; s < 8 + D; ++s) {
    if (s < 8) {
      xf[s % (D + 1)] = lds128(xa + 64 * s);
#pragma unroll
      for (int ct = 0; ct < NCT; ++ct) { const v2u r0 = ldstr(a[ct] + 4096 * s), r1 = ldstr(a[ct] + 4096 * s + 512); wf[s % (D + 1)][ct] = v4u{r0[0], r0[1], r1[0], r1[1]}; }
    }
    SB();
    if (s >= D) {
#pragma unroll
      for (int ct = 0; ct < NCT; ++ct) d[ct] = mfma16(wf[(s - D) % (D + 1)][ct], xf[(s - D) % (D + 1)], d[ct]);
    }
    SB();
  }
}

template <int NU, int LS, int WIN>
DI void b_stream(rsrc_t rk, rsrc_t rv, rsrc_t ws, int ub, int w, int lane, int nwaves, int obase, int mlbase ST_ARG) {
  const int c = lane & 15, g = lane >> 4;
  const lds_t my = B_KS + w * 8192;
  constexpr int NL = 8 * NU;
  v4u kr[NL], vr[NL], pf[NU];
  f32x4 acc[8];
#pragma unroll
  for (int k = 0; k < 8; ++k) acc[k] = f32x4{0.f, 0.f, 0.f, 0.f};
#pragma unroll
  for (int n = 0; n < NL + WQK; ++n) {
    if (n < WIN) vmcnt<WIN - 1>();
    if (n < NL) kr[n] = ld_nt(rk, lane * 16, ub + n * LS);
    else vr[n - NL] = ld_nt(rv, lane * 16, ub + (n - NL) * LS);
    SB();
    const int t = n + 1 - WIN;
    if (t >= 0 && t < NL) { k_write(kr[t], t & 7, my, c, g); if ((t & 7) == 7) k_read(kr + (t - 7), my, c, g); }
    SB();
  }
  STAMP(3);
  lds_wait(B_CNT + 8, 1u);
  const unsigned tag = __builtin_amdgcn_readfirstlane(*LP(unsigned, B_EP)) + 1u;
  STAMP(4);
  b_scores<NU>(kr, pf, ws, w, lane, nwaves, mlbase, tag);
  SB();
  STAMP(5);
#pragma unroll
  for (int n = WQK; n < NL; ++n) {
    vmcnt<WIN - 1>();
    vr[n] = ld_nt(rv, lane * 16, ub + n * LS);
    SB();
    if (n == 13) { b_pv(pf[0], vr, acc); SB(); }
  }
  STAMP(6);
  if (NU > 1) { b_pv(pf[NU - 2], vr + 8 * (NU - 2), acc); SB(); }
  b_pv(pf[NU - 1], vr + 8 * (NU - 1), acc);
  SB();
  STAMP(7);
  b_merge(acc, ws, w, lane, nwaves, obase, tag ST_PASS);
}
DI void fetch_qb(rsrc_t ws, int src, unsigned tag, int lane) {
  v4u f0, f1;
  for (unsigned spins = 0;; ++spins) {
    f0 = ld_sc(ws, lane * 16, src); f1 = ld_sc(ws, lane * 16 + 1024, src);
    if (all_ok(tags_ok(f0, tag) && tags_ok(f1, tag)) || spins >= SPIN_CAP) break;
    __builtin_amdgcn_s_sleep(4);
  }
  *LP(v2u, B_QB + lane * 8) = v2u{f0[0], f0[2]};
  *LP(v2u, B_QB + 512 + lane * 8) = v2u{f1[0], f1[2]};
  asm volatile("s_waitcnt lgkmcnt(0)" ::: "memory");
  if (lane == 0) *(volatile LAS unsigned*)(size_t)(B_CNT + 8) = 1u;
}

DI void role_b(const Params& p, int x, int j, int lane, int w) {
  STAMP_DECL
  STAMP(0);
  const int type = j % 3, k3 = j / 3, b = j / 12, sp = j % 12;
  const bool tq = type != 2;
  const int c = lane & 15, g = lane >> 4;
  const int col = 8 * k3 + ((x + k3) & 7);
  const rsrc_t ws = mk(p.ws, WS_BYTES);
  const rsrc_t rw = mk(tq ? p.wqa : p.wqb, 512 * 8192);
  const int sb = (type & 1) * 256 * 8192 + col * 128;
  const int vn0 = (lane >> 3) * 8192 + (((lane & 7) ^ ((g & 1) << 1)) << 4), vn1 = vn0 ^ 64;
  auto dma_w = [&](int n) {
    dma_nt(rw, (n & 1) ? vn1 : vn0, sb + ((n >> 2) * 256 + 32 * w + 8 * (n & 3)) * 8192, (n >> 2) * 32768 + (4 * w + (n & 3)) * 1024);
  };
  const int hq = col >> 1, hu = (hq & ~3) | ((hq + 2) & 3), cq = 2 * (col & 1) + type;
  constexpr int DUMMY = 71680;
  if (tq) {
#pragma unroll
    for (int n = 0; n < 4; ++n) dma_w(n);
  } else {
    const int ka = (3 - x - ((unsigned)(size_t)p.wqa >> 7)) & 7, kb = (3 - x - ((unsigned)(size_t)p.wqb >> 7)) & 7;
    const int vp = (lane >> 3) * 8192 + (lane & 7) * 16, sp0 = (8 * k3 + w) * 8 * 8192;
    dma_df(mk(p.wqa, 512 * 8192), vp, sp0 + (8 * ka + ((x + ka) & 7)) * 128, DUMMY);
    dma_df(rw, vp, sp0 + (8 * kb + ((x + kb) & 7)) * 128, DUMMY);
    dma_w(0); dma_w(1);
  }
  if (w == 0) {
    const rsrc_t rx = mk(tq ? p.xa : p.xb, 2048);
    const int vx = (lane >> 5) * 1024 + (lane & 31) * 16;
    dma_df(rx, vx, (type & 1) * 512, B_X); dma_df(rx, vx, 512, B_X + 1024);
    dma_sc(ws, 0, WS_EPOCH, B_EP);
    if (lane < 8) *LP(unsigned, B_CNT + lane * 4) = 0u;
    if (lane < 4) *LP(f32x2, B_ML + lane * 8) = f32x2{-INFINITY, 0.f};
    *LP(f32x4, B_KS + lane * 32) = f32x4{0.f, 0.f, 0.f, 0.f}; *LP(f32x4, B_KS + lane * 32 + 16) = f32x4{0.f, 0.f, 0.f, 0.f};
  }
  const rsrc_t rk = mk(p.kc, 16 * 8192 * 256), rv = mk(p.vc, 16 * 8192 * 256);
  const int pbase = (b * 8 + x) * 8192 * 256;
  const int obase = WS_BO + 4 * x * 12288 + sp * 1024 + b * 512, mlbase = WS_BML + x * 4096 + b * 2048 + sp * 128;
  if (!tq) {
    bar();
#pragma unroll
    for (int n = 2; n < 8; ++n) { VMCNT(4); dma_w(n); }
    VMCNT(4);
    if (w > 0) {
      lds_signal(B_CNT + 12, lane);
      STAMP(1);
      const rsrc_t rc = mk(p.ckv, 2 * 8192 * 1024);
      const int cb = ((x & 1) * 8192 + (x >> 1) * 2048 + k3 * 256) * 1024;
#pragma unroll
      for (int s = 0; s < PFN; ++s) {
        VMCNT(4);
        if (s == 4) { lds_signal(B_CNT + 16, lane); STAMP(2); }
        dma_df(rc, lane * 16, cb + ((256 - 7 * PFN + 7 * s + w - 1 + 32 * k3) & 255) * 1024, DUMMY);
      }
    }
    STAMP(12);
    {
      int cs = 0;
#pragma unroll
      for (int i = 0; i < 8; ++i) { const int cc = 2 * (4 * ((i & 6) + (x & 1)) + (((x >> 1) + (i >> 1)) & 3)) + (i & 1); if (((cc + ((unsigned)(size_t)p.wuv >> 7)) & 7) == 3) cs = cc; }
      if (w > 0) dma_df(mk(p.wuv, 512 * 8192), (lane >> 3) * 8192 + (lane & 7) * 16, (8 * k3 + w) * 8 * 8192 + cs * 128, DUMMY);
    }
  } else {
    bar();
    const rsrc_t ru = mk(p.wuk, 512 * 8192);
    const int vt = g * 8192 + ((c ^ (g | ((w & 1) << 3))) << 4);
#pragma unroll
    for (int n = 0; n < 4; ++n) { VMCNT(3); dma_nt(ru, vt ^ ((n >> 1) << 6), (cq * 128 + 16 * w + 4 * n) * 8192 + hu * 256, 32768 + (4 * w + n) * 1024); }
    VMCNT(4);
    bar();
    STAMP(1);
    if (w >= 4) {
      const unsigned tag = __builtin_amdgcn_readfirstlane(*LP(unsigned, B_EP)) + 1u;
      f32x4 d = {0.f, 0.f, 0.f, 0.f};
      gemv_n<1>(&d, w - 4, 0, B_X, c, g);
      if (c < 4) st_sc(v4u{fu(c < 2 ? d[0] : d[2]), tag, fu(c < 2 ? d[1] : d[3]), tag}, ws, (c & 1) * 1024 + g * 32 + (c >> 1) * 16, WS_QA + hq * 4096 + type * 2048 + (col & 1) * 512 + (w - 4) * 128);
      VMCNT(1);
    } else {
      VMCNT(0);
    }
    bar();
    STAMP(12);
    if (w > 0) { lds_wait(B_CNT + 20, 1u); STAMP(9); }
  }
  if (w > 0) {
    b_stream<3, 7168, W1>(rk, rv, ws, pbase + 21 * sp * 8192 + (w - 1) * 1024, w, lane, tq ? 7 : 8, obase, mlbase ST_PASS);
    STAMP_FLUSH();
    return;
  }
  if (!tq) {
    lds_wait(B_CNT + 12, 7u);
    STAMP(1);
    const unsigned tag = __builtin_amdgcn_readfirstlane(*LP(unsigned, B_EP)) + 1u;
    f32x4 d[4];
#pragma unroll
    for (int ct = 0; ct < 4; ++ct) d[ct] = f32x4{0.f, 0.f, 0.f, 0.f};
#pragma unroll 1
    for (int part = 0; part < 2; ++part) {
      if (part) { VMCNT(0); lds_wait(B_CNT + 16, 7u); STAMP(2); }
      gemv_n<4>(d, 0, part * 32768, B_X + part * 1024, c, g);
    }
    const int ct = (c >> 1) & 3;
    const f32x4 dd = ct == 0 ? d[0] : ct == 1 ? d[1] : ct == 2 ? d[2] : d[3];
    if (c < 8) st_sc(v4u{pk(dd[0], dd[1]), tag, pk(dd[2], dd[3]), tag}, ws, (c & 1) * 16384 + ct * 64 + g * 16, WS_QB + col * 256);
    STAMP(11);
    fetch_qb(ws, WS_QB + (b * 8 + x) * 2048, tag, lane);
    STAMP(13);
    b_stream<1, 1024, WCTL>(rk, rv, ws, pbase + (252 + (k3 & 3)) * 8192, 0, lane, 8, obase, mlbase ST_PASS);
    lds_wait(B_CNT + 4, 8u);
    STAMP(9);
    const int slice = 8 * x + k3;
    final_reduce<4>(ws, WS_OPB + slice * 4096, slice, p.out, 512, tag, lane);
    STAMP(10);
    if (slice == 0) {
      for (unsigned spins = 0;; ++spins) {
        const v4u f = ld_sc(ws, lane * 512, WS_AML);
        if (all_ok(tags_ok(f, tag)) || spins >= SPIN_CAP) break;
        __builtin_amdgcn_s_sleep(1);
      }
      if (lane == 0) __builtin_amdgcn_raw_buffer_store_b32(tag, ws, 0, WS_EPOCH, AUX_SC1);
    }
    STAMP_FLUSH();
    return;
  }
  const unsigned tag = __builtin_amdgcn_readfirstlane(*LP(unsigned, B_EP)) + 1u;
  {
    v4u wf[32];
    {
      const int r0 = 32768 + (8 * (c >> 2) + (c & 3)) * 256;
#pragma unroll
      for (int s = 0; s < 4; ++s) {
        int a = r0 + (((4 * s + g) ^ c) << 4);
        PIN(a);
#pragma unroll
        for (int u = 0; u < 8; ++u) wf[4 * u + s] = lds128(a + (u >> 1) * 8192 + (u & 1) * 1024);
      }
    }
    v4u f[4];
    for (unsigned spins = 0;; ++spins) {
#pragma unroll
      for (int n = 0; n < 4; ++n) f[n] = ld_sc(ws, lane * 16 + n * 1024, WS_QA + hu * 4096);
      if (all_ok(tags_ok(f[0], tag) && tags_ok(f[1], tag) && tags_ok(f[2], tag) && tags_ok(f[3], tag)) || spins >= SPIN_CAP) break;
      asm volatile("" ::: "memory");
    }
    STAMP(2);
    *LP(unsigned, B_QA + lane * 4) = pk(uf(f[0][0]) + uf(f[2][0]), uf(f[0][2]) + uf(f[2][2]));
    *LP(unsigned, B_QA + 256 + lane * 4) = pk(uf(f[1][0]) + uf(f[3][0]), uf(f[1][2]) + uf(f[3][2]));
    v4u qf[4];
#pragma unroll
    for (int s = 0; s < 4; ++s) qf[s] = lds128(B_QA + (c & 1) * 256 + 64 * s + 16 * g);
    f32x4 e[8];
#pragma unroll
    for (int u = 0; u < 8; ++u) e[u] = f32x4{0.f, 0.f, 0.f, 0.f};
#pragma unroll
    for (int s = 0; s < 4; ++s)
#pragma unroll
      for (int u = 0; u < 8; ++u) e[u] = mfma16(wf[4 * u + s], qf[s], e[u]);
    const int ct = (c >> 1) & 3;
    const f32x4 d0 = ct == 0 ? e[0] : ct == 1 ? e[2] : ct == 2 ? e[4] : e[6], d1 = ct == 0 ? e[1] : ct == 1 ? e[3] : ct == 2 ? e[5] : e[7];
    if (c < 8) st_sc(v4u{pk(d0[0], d0[1]), pk(d0[2], d0[3]), pk(d1[0], d1[1]), pk(d1[2], d1[3])}, ws, (c & 1) * 32768 + ct * 64 + g * 16, WS_QABS + hu * 1024 + cq * 256);
    STAMP(3);
    VMCNT(0);
    if (lane == 0) __builtin_amdgcn_raw_buffer_store_b64(v2u{tag, ~tag}, ws, WS_QFLAG + hu * FSTR + cq * 8, 0, AUX_SC1);
    STAMP(4);
    if (lane == 0) *(volatile LAS unsigned*)(size_t)(B_CNT + 20) = 1u;
  }
  for (int k = 0; k < QBDELAY; ++k) __builtin_amdgcn_s_sleep(8);
  STAMP(5);
  fetch_qb(ws, WS_QB + (b * 8 + x) * 2048, tag, lane);
  STAMP(6);
  {
    const int k2 = 2 * k3 + type, h = 4 * x + (k2 >> 2), cg = k2 & 3;
    const rsrc_t ro = mk(p.wob, 4096 * 1024);
    v4u wo[32];
#pragma unroll
    for (int n = 0; n < 32; ++n) { wo[n] = ld_nt(ro, g * 8192 + c * 16, (h * 128 + 32 * (n >> 3) + (n & 7)) * 1024 + cg * 256); vmcnt<WCTL - 1>(); SB(); }
#pragma unroll
    for (int t = 0; t < 4; ++t) transpose8(wo + 8 * t);
#pragma unroll
    for (int n = 0; n < 32; ++n) PIN(wo[n]);
    STAMP(7);
    lds_wait(B_CNT, 7u);
    float wt[12];
    {
      const bool act = (lane & 31) < 12;
      v4u f;
      for (unsigned spins = 0;; ++spins) {
        f = ld_sc(ws, (lane >> 5) * 2048 + (act ? (lane & 31) : 0) * 128, WS_BML + x * 4096 + (h & 3) * 16);
        if (all_ok(tags_ok(f, tag)) || spins >= SPIN_CAP) break;
        __builtin_amdgcn_s_sleep(4);
      }
      const float mm = uf(f[0]), M = row_max(mm);
      const float e = act ? ex2((mm - M) * C2) : 0.f, Lt = row_sum(e * uf(f[2]));
      if ((lane & 16) == 0) *LP(float, B_WT + ((lane >> 5) * 16 + c) * 4) = e / Lt;
      asm volatile("" ::: "memory");
#pragma unroll
      for (int s = 0; s < 3; ++s) {
        const f32x4 t = *LP(f32x4, B_WT + (lane >> 5) * 64 + s * 16);
        wt[4 * s] = t[0]; wt[4 * s + 1] = t[1]; wt[4 * s + 2] = t[2]; wt[4 * s + 3] = t[3];
      }
    }
    STAMP(8);
    lds_wait(B_CNT + 4, 7u);
    float o[4];
    for (unsigned spins = 0;; ++spins) {
      v4u v[12];
#pragma unroll
      for (int s = 0; s < 12; ++s) v[s] = ld_sc(ws, lane * 16 + (s & 3) * 1024, WS_BO + h * 12288 + (s >> 2) * 4096);
      bool ok = true;
      o[0] = o[1] = o[2] = o[3] = 0.f;
#pragma unroll
      for (int s = 0; s < 12; ++s) {
        ok = ok && tags_ok(v[s], tag);
        o[0] = fmaf(wt[s], bflo(v[s][0]), o[0]); o[1] = fmaf(wt[s], bfhi(v[s][0]), o[1]);
        o[2] = fmaf(wt[s], bflo(v[s][2]), o[2]); o[3] = fmaf(wt[s], bfhi(v[s][2]), o[3]);
      }
      if (all_ok(ok) || spins >= SPIN_CAP) break;
      asm volatile("" ::: "memory");
    }
    STAMP(9);
    *LP(v2u, B_QA + lane * 8) = v2u{pk(o[0], o[1]), pk(o[2], o[3])};
    asm volatile("" ::: "memory");
    out_gemv<4>(wo, B_QA, ws, WS_OPB + cg * 16 * 4096 + h * 128, 4096, tag, lane);
    STAMP(10);
  }
  STAMP_FLUSH();
}

__global__ void __launch_bounds__(512, 2) k_fused(Params p) {
  const int lane = threadIdx.x & 63, w = __builtin_amdgcn_readfirstlane(threadIdx.x >> 6);
  const int x = blockIdx.x & 7, i = blockIdx.x >> 3;
  if (w >= 6 && i + 32 * (7 - w) < CODE_KB) {
    const rsrc_t rcode = mk((const void*)(__builtin_amdgcn_s_getpc() & ~255ull), CODE_KB * 1024);
    dma_df(rcode, lane * 16, (i + 32 * (7 - w)) * 1024, i >= 24 ? 2048 : 71680);
  }
  if (i >= 24) role_a(p, x, i - 24, lane, w);
  else role_b(p, x, i, lane, w);
}

__global__ void k_pre(unsigned* st) { unsigned long long t0 = __builtin_amdgcn_s_memrealtime(); while (__builtin_amdgcn_s_memrealtime() - t0 < 1000) __builtin_amdgcn_s_sleep(1);
  if (threadIdx.x == 0) st[2048 * 16] = (unsigned)__builtin_amdgcn_s_memrealtime(); }
__global__ void k_amp(const unsigned* st, int mult) {
  const unsigned tin = (unsigned)__builtin_amdgcn_s_memrealtime();
  const unsigned ref = st[0];
  int mn = 0x7fffffff, mx = -0x7fffffff;
  for (int i = threadIdx.x; i < 2048 * 16; i += 256) { const int k = i & 15; const unsigned v = st[i]; if (!v) continue; const int d = (int)(v - ref);
    if (k == 0) mn = d < mn ? d : mn;
    if (k < 15) mx = d > mx ? d : mx; }
  __shared__ int a[256], c[256];
  a[threadIdx.x] = mn; c[threadIdx.x] = mx; __syncthreads();
  if (threadIdx.x == 0) { for (int i = 0; i < 256; ++i) { mn = a[i] < mn ? a[i] : mn; mx = c[i] > mx ? c[i] : mx; }
    unsigned long long dd = (unsigned long long)((int)(tin - ref) - mx) * mult; if (dd > 200000) dd = 200000;
    unsigned long long t0 = __builtin_amdgcn_s_memrealtime();
    while (__builtin_amdgcn_s_memrealtime() - t0 < dd) __builtin_amdgcn_s_sleep(1); }
}
extern "C" void kernel_launch(void* const* d_in, const int* in_sizes, int n_in, void* d_out, int out_size, void* d_ws, size_t ws_size, hipStream_t stream) {
  Params p;
  p.xa = d_in[0]; p.ckv = d_in[1]; p.wqa = d_in[2]; p.wuk = d_in[3]; p.wuv = d_in[4]; p.woa = d_in[5];
  p.xb = d_in[6]; p.kc = d_in[7]; p.vc = d_in[8]; p.wqb = d_in[9]; p.wob = d_in[10];
  p.out = d_out; p.ws = d_ws;
  (void)hipFuncSetAttribute((const void*)k_fused, hipFuncAttributeMaxDynamicSharedMemorySize, 163840);
  k_pre<<<1, 64, 0, stream>>>((unsigned*)((char*)d_ws + WS_ST));
  k_fused<<<256, 512, 163840, stream>>>(p);
  k_amp<<<1, 256, 0, stream>>>((const unsigned*)((char*)d_ws + WS_ST), 0);
}
```

```cpp
#include <hip/hip_runtime.h>
#include <hip/hip_bf16.h>

#define LAS __attribute__((address_space(3)))
#define DI __device__ __forceinline__
typedef unsigned v4u __attribute__((ext_vector_type(4)));
typedef unsigned v2u __attribute__((ext_vector_type(2)));
typedef float f32x4 __attribute__((ext_vector_type(4)));
typedef float f32x2 __attribute__((ext_vector_type(2)));
typedef float f32x16 __attribute__((ext_vector_type(16)));
typedef __bf16 bf16x8 __attribute__((ext_vector_type(8)));
typedef __bf16 bf16x2 __attribute__((ext_vector_type(2)));
typedef short v4s __attribute__((ext_vector_type(4)));
typedef unsigned short u16;
typedef __amdgpu_buffer_rsrc_t rsrc_t;
typedef unsigned lds_t;
#define LP(T, a) ((LAS T*)(size_t)(unsigned)(a))

constexpr int AUX_NT = 2, AUX_SC1 = 16;
constexpr float C2 = 0.088388347648318440f * 1.4426950408889634f;
constexpr unsigned SPIN_CAP = 1u << 19, LSPIN = 1u << 22;

constexpr int WS_EPOCH = 0;
constexpr int WS_QFLAG = 16384;
constexpr int WS_QA = 1572864;
constexpr int WS_QB = 65536;
constexpr int WS_AML = 98304;
constexpr int WS_QABS = 131072;
constexpr int WS_BML = 196608;
constexpr int WS_OPB = 262144;
constexpr int WS_OPA = 524288;
constexpr int WS_BO = 1048576;
constexpr int WS_AO = 2097152;
constexpr int WS_BYTES = 4194304;

struct Params {
  const void *xa, *ckv, *wqa, *wuk, *wuv, *woa, *xb, *kc, *vc, *wqb, *wob;
  void *out, *ws;
};

DI rsrc_t mk(const void* p, int bytes) { return __builtin_amdgcn_make_buffer_rsrc((void*)p, (short)0, bytes, 0x00020000); }
DI v4u ld_nt(rsrc_t r, int voff, int soff) { return __builtin_amdgcn_raw_buffer_load_b128(r, voff, soff, AUX_NT); }
DI v4u ld_sc(rsrc_t r, int voff, int soff) { return __builtin_amdgcn_raw_buffer_load_b128(r, voff, soff, AUX_SC1); }
DI void st_sc(v4u v, rsrc_t r, int voff, int soff) { __builtin_amdgcn_raw_buffer_store_b128(v, r, voff + soff, 0, AUX_SC1); }
DI void dma_nt(rsrc_t r, int voff, int soff, unsigned lds) {
  asm volatile("s_mov_b32 m0, %0\n\ts_nop 0\n\tbuffer_load_dwordx4 %1, %2, %3 offen nt lds" ::"s"(lds), "v"(voff), "s"(r), "s"(soff) : "memory");
}
DI void dma_df(rsrc_t r, int voff, int soff, unsigned lds) {
  asm volatile("s_mov_b32 m0, %0\n\ts_nop 0\n\tbuffer_load_dwordx4 %1, %2, %3 offen lds" ::"s"(lds), "v"(voff), "s"(r), "s"(soff) : "memory");
}
DI void dma_sc(rsrc_t r, int voff, int soff, unsigned lds) {
  asm volatile("s_mov_b32 m0, %0\n\ts_nop 0\n\tbuffer_load_dwordx4 %1, %2, %3 offen sc1 lds" ::"s"(lds), "v"(voff), "s"(r), "s"(soff) : "memory");
}
#define VMCNT(n) asm volatile("s_waitcnt vmcnt(" #n ")" ::: "memory")
template <int N> DI void vmcnt() { asm volatile("s_waitcnt vmcnt(%0)" ::"i"(N) : "memory"); }
#define SB() __builtin_amdgcn_sched_barrier(0)
#define PIN(x) asm volatile("" : "+v"(x))
#define W1 6
#define PFN 8
#define PFWV 1
#define WQK 8
#define WCTL 5
#define WBETA 7
#define WO_EARLY 8
#define WV_S 5
#define FSTR 32
#define QDELAY 0
#define ILV 1
#define QBDELAY 0
#define PA 0
#define PB 0
#define PSTEP 4
#define WO1 0
#define WO_LATE 7
#define QBT 700
#define SL_AML 8
#define D_AML 0
#define SL_BO 0
#define SL_FA 0
#define D_FA 30
#define PRE 3
#define GI 2
#define TWO 0
#define TWA 0
#define HOLD 0
#define CODE_KB 33
DI void bar() { asm volatile("s_waitcnt lgkmcnt(0)\n\ts_barrier" ::: "memory"); }
DI float bflo(unsigned u) { return __builtin_bit_cast(float, u << 16); }
DI float bfhi(unsigned u) { return __builtin_bit_cast(float, u & 0xffff0000u); }
DI unsigned fu(float f) { return __builtin_bit_cast(unsigned, f); }
DI float uf(unsigned u) { return __builtin_bit_cast(float, u); }
DI unsigned pk(float lo, float hi) { f32x2 v = {lo, hi}; return __builtin_bit_cast(unsigned, __builtin_convertvector(v, bf16x2)); }
DI float ex2(float x) { return __builtin_amdgcn_exp2f(x); }
DI f32x4 mfma16(v4u a, v4u b, f32x4 c) { return __builtin_amdgcn_mfma_f32_16x16x32_bf16(__builtin_bit_cast(bf16x8, a), __builtin_bit_cast(bf16x8, b), c, 0, 0, 0); }
DI f32x16 mfma32(v4u a, v4u b, f32x16 c) { return __builtin_amdgcn_mfma_f32_32x32x16_bf16(__builtin_bit_cast(bf16x8, a), __builtin_bit_cast(bf16x8, b), c, 0, 0, 0); }
DI v4u lds128(lds_t p) { return *LP(v4u, p); }
DI v2u ldstr(lds_t p) { return __builtin_bit_cast(v2u, __builtin_amdgcn_ds_read_tr16_b64_v4i16(LP(v4s, p))); }
DI float x16_max(float x) { auto s = __builtin_amdgcn_permlane16_swap(fu(x), fu(x), false, false); return fmaxf(uf(s[0]), uf(s[1])); }
DI float x32_max(float x) { auto s = __builtin_amdgcn_permlane32_swap(fu(x), fu(x), false, false); return fmaxf(uf(s[0]), uf(s[1])); }
DI float x16_sum(float x) { auto s = __builtin_amdgcn_permlane16_swap(fu(x), fu(x), false, false); return uf(s[0]) + uf(s[1]); }
DI float x32_sum(float x) { auto s = __builtin_amdgcn_permlane32_swap(fu(x), fu(x), false, false); return uf(s[0]) + uf(s[1]); }
template <int CTRL> DI float dpp(float x) { return uf(__builtin_amdgcn_mov_dpp(fu(x), CTRL, 0xf, 0xf, true)); }
constexpr int XOR1 = 0xB1, XOR2 = 0x4E, ROR4 = 0x124, ROR8 = 0x128;
DI float row_max(float x) { x = fmaxf(x, dpp<XOR1>(x)); x = fmaxf(x, dpp<XOR2>(x)); x = fmaxf(x, dpp<ROR4>(x)); return fmaxf(x, dpp<ROR8>(x)); }
DI float row_sum(float x) { x += dpp<XOR1>(x); x += dpp<XOR2>(x); x += dpp<ROR4>(x); return x + dpp<ROR8>(x); }
DI unsigned fsw(unsigned row) { return ((row & 3) << 2) | ((row >> 2) & 3); }
DI bool tags_ok(v4u v, unsigned tag) { return v[1] == tag && v[3] == tag; }
DI bool all_ok(bool ok) { return __builtin_amdgcn_ballot_w64(!ok) == 0ull; }
DI v4u tfrag(const v4u* r, int i) {
  v4u o;
#pragma unroll
  for (int t = 0; t < 4; ++t) o[t] = __builtin_amdgcn_perm(r[2 * t + 1][i >> 1], r[2 * t][i >> 1], (i & 1) ? 0x07060302u : 0x05040100u);
  return o;
}
DI void transpose8(v4u* r) {
  v4u o[8];
#pragma unroll
  for (int i = 0; i < 8; ++i) o[i] = tfrag(r, i);
#pragma unroll
  for (int i = 0; i < 8; ++i) r[i] = o[i];
}

#define STAMP(i)
#define STAMP_DECL
#define STAMP_FLUSH()

template <int N, int SLEEP> DI void gather(v4u* v, rsrc_t r, int voff, int soff, unsigned tag) {
  unsigned a, t, u, cnt;
  if constexpr (N == 1) asm volatile(
      "s_mov_b32 %[cnt], %[cap]\n"
      "buffer_load_dwordx4 v[252:255], %[vo], %[rs], %[s0] offen offset:0 sc1\n"
      ".Lg_loop%=:\n"
      "s_waitcnt vmcnt(0)\n"
      "v_bitop3_b32 %[a], v253, v255, %[tag] bitop3:0x7e\n"
      "v_cmp_ne_u32 vcc, 0, %[a]\n"
      "s_cbranch_vccz .Lg_done%=\n"
      "s_sub_u32 %[cnt], %[cnt], 1\n"
      "s_cbranch_scc1 .Lg_done%=\n"
      "s_sleep %[sl]\n"
      "s_mov_b64 exec, vcc\n"
      "buffer_load_dwordx4 v[252:255], %[vo], %[rs], %[s0] offen offset:0 sc1\n"
      "s_mov_b64 exec, -1\n"
      "s_branch .Lg_loop%=\n"
      ".Lg_done%=:\n"
      : "=&{v[252:255]}"(v[0]), [a] "=&v"(a), [t] "=&v"(t), [u] "=&v"(u), [cnt] "=&s"(cnt)
      : [vo] "v"(voff), [rs] "s"(r), [tag] "s"(tag), [s0] "s"(soff + 0), [cap] "i"(SPIN_CAP), [sl] "i"(SLEEP) : "vcc", "scc", "memory");
  if constexpr (N == 2) asm volatile(
      "s_mov_b32 %[cnt], %[cap]\n"
      "buffer_load_dwordx4 v[248:251], %[vo], %[rs], %[s0] offen offset:0 sc1\n"
      "buffer_load_dwordx4 v[252:255], %[vo], %[rs], %[s0] offen offset:1024 sc1\n"
      ".Lg_loop%=:\n"
      "s_waitcnt vmcnt(0)\n"
      "v_bitop3_b32 %[a], v249, v251, %[tag] bitop3:0x7e\n"
      "v_bitop3_b32 %[t], v253, v255, %[tag] bitop3:0x7e\n"
      "v_or_b32 %[a], %[a], %[t]\n"
      "v_cmp_ne_u32 vcc, 0, %[a]\n"
      "s_cbranch_vccz .Lg_done%=\n"
      "s_sub_u32 %[cnt], %[cnt], 1\n"
      "s_cbranch_scc1 .Lg_done%=\n"
      "s_sleep %[sl]\n"
      "v_bitop3_b32 %[t], v249, v251, %[tag] bitop3:0x7e\n"
      "v_cmp_ne_u32 vcc, 0, %[t]\n"
      "s_mov_b64 exec, vcc\n"
      "buffer_load_dwordx4 v[248:251], %[vo], %[rs], %[s0] offen offset:0 sc1\n"
      "s_mov_b64 exec, -1\n"
      "v_bitop3_b32 %[t], v253, v255, %[tag] bitop3:0x7e\n"
      "v_cmp_ne_u32 vcc, 0, %[t]\n"
      "s_mov_b64 exec, vcc\n"
      "buffer_load_dwordx4 v[252:255], %[vo], %[rs], %[s0] offen offset:1024 sc1\n"
      "s_mov_b64 exec, -1\n"
      "s_branch .Lg_loop%=\n"
      ".Lg_done%=:\n"
      : "=&{v[248:251]}"(v[0]), "=&{v[252:255]}"(v[1]), [a] "=&v"(a), [t] "=&v"(t), [u] "=&v"(u), [cnt] "=&s"(cnt)
      : [vo] "v"(voff), [rs] "s"(r), [tag] "s"(tag), [s0] "s"(soff + 0), [cap] "i"(SPIN_CAP), [sl] "i"(SLEEP) : "vcc", "scc", "memory");
  if constexpr (N == 4) asm volatile(
      "s_mov_b32 %[cnt], %[cap]\n"
      "buffer_load_dwordx4 v[240:243], %[vo], %[rs], %[s0] offen offset:0 sc1\n"
      "buffer_load_dwordx4 v[244:247], %[vo], %[rs], %[s0] offen offset:1024 sc1\n"
      "buffer_load_dwordx4 v[248:251], %[vo], %[rs], %[s0] offen offset:2048 sc1\n"
      "buffer_load_dwordx4 v[252:255], %[vo], %[rs], %[s0] offen offset:3072 sc1\n"
      ".Lg_loop%=:\n"
      "s_waitcnt vmcnt(0)\n"
      "v_bitop3_b32 %[a], v241, v243, %[tag] bitop3:0x7e\n"
      "v_bitop3_b32 %[t], v245, v247, %[tag] bitop3:0x7e\n"
      "v_bitop3_b32 %[u], v249, v251, %[tag] bitop3:0x7e\n"
      "v_or3_b32 %[a], %[a], %[t], %[u]\n"
      "v_bitop3_b32 %[t], v253, v255, %[tag] bitop3:0x7e\n"
      "v_or_b32 %[a], %[a], %[t]\n"
      "v_cmp_ne_u32 vcc, 0, %[a]\n"
      "s_cbranch_vccz .Lg_done%=\n"
      "s_sub_u32 %[cnt], %[cnt], 1\n"
      "s_cbranch_scc1 .Lg_done%=\n"
      "s_sleep %[sl]\n"
      "v_bitop3_b32 %[t], v241, v243, %[tag] bitop3:0x7e\n"
      "v_cmp_ne_u32 vcc, 0, %[t]\n"
      "s_mov_b64 exec, vcc\n"
      "buffer_load_dwordx4 v[240:243], %[vo], %[rs], %[s0] offen offset:0 sc1\n"
      "s_mov_b64 exec, -1\n"
      "v_bitop3_b32 %[t], v245, v247, %[tag] bitop3:0x7e\n"
      "v_cmp_ne_u32 vcc, 0, %[t]\n"
      "s_mov_b64 exec, vcc\n"
      "buffer_load_dwordx4 v[244:247], %[vo], %[rs], %[s0] offen offset:1024 sc1\n"
      "s_mov_b64 exec, -1\n"
      "v_bitop3_b32 %[t], v249, v251, %[tag] bitop3:0x7e\n"
      "v_cmp_ne_u32 vcc, 0, %[t]\n"
      "s_mov_b64 exec, vcc\n"
      "buffer_load_dwordx4 v[248:251], %[vo], %[rs], %[s0] offen offset:2048 sc1\n"
      "s_mov_b64 exec, -1\n"
      "v_bitop3_b32 %[t], v253, v255, %[tag] bitop3:0x7e\n"
      "v_cmp_ne_u32 vcc, 0, %[t]\n"
      "s_mov_b64 exec, vcc\n"
      "buffer_load_dwordx4 v[252:255], %[vo], %[rs], %[s0] offen offset:3072 sc1\n"
      "s_mov_b64 exec, -1\n"
      "s_branch .Lg_loop%=\n"
      ".Lg_done%=:\n"
      : "=&{v[240:243]}"(v[0]), "=&{v[244:247]}"(v[1]), "=&{v[248:251]}"(v[2]), "=&{v[252:255]}"(v[3]), [a] "=&v"(a), [t] "=&v"(t), [u] "=&v"(u), [cnt] "=&s"(cnt)
      : [vo] "v"(voff), [rs] "s"(r), [tag] "s"(tag), [s0] "s"(soff + 0), [cap] "i"(SPIN_CAP), [sl] "i"(SLEEP) : "vcc", "scc", "memory");
  if constexpr (N == 8) asm volatile(
      "s_mov_b32 %[cnt], %[cap]\n"
      "buffer_load_dwordx4 v[224:227], %[vo], %[rs], %[s0] offen offset:0 sc1\n"
      "buffer_load_dwordx4 v[228:231], %[vo], %[rs], %[s0] offen offset:1024 sc1\n"
      "buffer_load_dwordx4 v[232:235], %[vo], %[rs], %[s0] offen offset:2048 sc1\n"
      "buffer_load_dwordx4 v[236:239], %[vo], %[rs], %[s0] offen offset:3072 sc1\n"
      "buffer_load_dwordx4 v[240:243], %[vo], %[rs], %[s1] offen offset:0 sc1\n"
      "buffer_load_dwordx4 v[244:247], %[vo], %[rs], %[s1] offen offset:1024 sc1\n"
      "buffer_load_dwordx4 v[248:251], %[vo], %[rs], %[s1] offen offset:2048 sc1\n"
      "buffer_load_dwordx4 v[252:255], %[vo], %[rs], %[s1] offen offset:3072 sc1\n"
      ".Lg_loop%=:\n"
      "s_waitcnt vmcnt(0)\n"
      "v_bitop3_b32 %[a], v225, v227, %[tag] bitop3:0x7e\n"
      "v_bitop3_b32 %[t], v229, v231, %[tag] bitop3:0x7e\n"
      "v_bitop3_b32 %[u], v233, v235, %[tag] bitop3:0x7e\n"
      "v_or3_b32 %[a], %[a], %[t], %[u]\n"
      "v_bitop3_b32 %[t], v237, v239, %[tag] bitop3:0x7e\n"
      "v_bitop3_b32 %[u], v241, v243, %[tag] bitop3:0x7e\n"
      "v_or3_b32 %[a], %[a], %[t], %[u]\n"
      "v_bitop3_b32 %[t], v245, v247, %[tag] bitop3:0x7e\n"
      "v_bitop3_b32 %[u], v249, v251, %[tag] bitop3:0x7e\n"
      "v_or3_b32 %[a], %[a], %[t], %[u]\n"
      "v_bitop3_b32 %[t], v253, v255, %[tag] bitop3:0x7e\n"
      "v_or_b32 %[a], %[a], %[t]\n"
      "v_cmp_ne_u32 vcc, 0, %[a]\n"
      "s_cbranch_vccz .Lg_done%=\n"
      "s_sub_u32 %[cnt], %[cnt], 1\n"
      "s_cbranch_scc1 .Lg_done%=\n"
      "s_sleep %[sl]\n"
      "v_bitop3_b32 %[t], v225, v227, %[tag] bitop3:0x7e\n"
      "v_cmp_ne_u32 vcc, 0, %[t]\n"
      "s_mov_b64 exec, vcc\n"
      "buffer_load_dwordx4 v[224:227], %[vo], %[rs], %[s0] offen offset:0 sc1\n"
      "s_mov_b64 exec, -1\n"
      "v_bitop3_b32 %[t], v229, v231, %[tag] bitop3:0x7e\n"
      "v_cmp_ne_u32 vcc, 0, %[t]\n"
      "s_mov_b64 exec, vcc\n"
      "buffer_load_dwordx4 v[228:231], %[vo], %[rs], %[s0] offen offset:1024 sc1\n"
      "s_mov_b64 exec, -1\n"
      "v_bitop3_b32 %[t], v233, v235, %[tag] bitop3:0x7e\n"
      "v_cmp_ne_u32 vcc, 0, %[t]\n"
      "s_mov_b64 exec, vcc\n"
      "buffer_load_dwordx4 v[232:235], %[vo], %[rs], %[s0] offen offset:2048 sc1\n"
      "s_mov_b64 exec, -1\n"
      "v_bitop3_b32 %[t], v237, v239, %[tag] bitop3:0x7e\n"
      "v_cmp_ne_u32 vcc, 0, %[t]\n"
      "s_mov_b64 exec, vcc\n"
      "buffer_load_dwordx4 v[236:239], %[vo], %[rs], %[s0] offen offset:3072 sc1\n"
      "s_mov_b64 exec, -1\n"
      "v_bitop3_b32 %[t], v241, v243, %[tag] bitop3:0x7e\n"
      "v_cmp_ne_u32 vcc, 0, %[t]\n"
      "s_mov_b64 exec, vcc\n"
      "buffer_load_dwordx4 v[240:243], %[vo], %[rs], %[s1] offen offset:0 sc1\n"
      "s_mov_b64 exec, -1\n"
      "v_bitop3_b32 %[t], v245, v247, %[tag] bitop3:0x7e\n"
      "v_cmp_ne_u32 vcc, 0, %[t]\n"
      "s_mov_b64 exec, vcc\n"
      "buffer_load_dwordx4 v[244:247], %[vo], %[rs], %[s1] offen offset:1024 sc1\n"
      "s_mov_b64 exec, -1\n"
      "v_bitop3_b32 %[t], v249, v251, %[tag] bitop3:0x7e\n"
      "v_cmp_ne_u32 vcc, 0, %[t]\n"
      "s_mov_b64 exec, vcc\n"
      "buffer_load_dwordx4 v[248:251], %[vo], %[rs], %[s1] offen offset:2048 sc1\n"
      "s_mov_b64 exec, -1\n"
      "v_bitop3_b32 %[t], v253, v255, %[tag] bitop3:0x7e\n"
      "v_cmp_ne_u32 vcc, 0, %[t]\n"
      "s_mov_b64 exec, vcc\n"
      "buffer_load_dwordx4 v[252:255], %[vo], %[rs], %[s1] offen offset:3072 sc1\n"
      "s_mov_b64 exec, -1\n"
      "s_branch .Lg_loop%=\n"
      ".Lg_done%=:\n"
      : "=&{v[224:227]}"(v[0]), "=&{v[228:231]}"(v[1]), "=&{v[232:235]}"(v[2]), "=&{v[236:239]}"(v[3]), "=&{v[240:243]}"(v[4]), "=&{v[244:247]}"(v[5]), "=&{v[248:251]}"(v[6]), "=&{v[252:255]}"(v[7]), [a] "=&v"(a), [t] "=&v"(t), [u] "=&v"(u), [cnt] "=&s"(cnt)
      : [vo] "v"(voff), [rs] "s"(r), [tag] "s"(tag), [s0] "s"(soff + 0), [s1] "s"(soff + 4096), [cap] "i"(SPIN_CAP), [sl] "i"(SLEEP) : "vcc", "scc", "memory");
  if constexpr (N == 12) asm volatile(
      "s_mov_b32 %[cnt], %[cap]\n"
      "buffer_load_dwordx4 v[208:211], %[vo], %[rs], %[s0] offen offset:0 sc1\n"
      "buffer_load_dwordx4 v[212:215], %[vo], %[rs], %[s0] offen offset:1024 sc1\n"
      "buffer_load_dwordx4 v[216:219], %[vo], %[rs], %[s0] offen offset:2048 sc1\n"
      "buffer_load_dwordx4 v[220:223], %[vo], %[rs], %[s0] offen offset:3072 sc1\n"
      "buffer_load_dwordx4 v[224:227], %[vo], %[rs], %[s1] offen offset:0 sc1\n"
      "buffer_load_dwordx4 v[228:231], %[vo], %[rs], %[s1] offen offset:1024 sc1\n"
      "buffer_load_dwordx4 v[232:235], %[vo], %[rs], %[s1] offen offset:2048 sc1\n"
      "buffer_load_dwordx4 v[236:239], %[vo], %[rs], %[s1] offen offset:3072 sc1\n"
      "buffer_load_dwordx4 v[240:243], %[vo], %[rs], %[s2] offen offset:0 sc1\n"
      "buffer_load_dwordx4 v[244:247], %[vo], %[rs], %[s2] offen offset:1024 sc1\n"
      "buffer_load_dwordx4 v[248:251], %[vo], %[rs], %[s2] offen offset:2048 sc1\n"
      "buffer_load_dwordx4 v[252:255], %[vo], %[rs], %[s2] offen offset:3072 sc1\n"
      ".Lg_loop%=:\n"
      "s_waitcnt vmcnt(0)\n"
      "v_bitop3_b32 %[a], v209, v211, %[tag] bitop3:0x7e\n"
      "v_bitop3_b32 %[t], v213, v215, %[tag] bitop3:0x7e\n"
      "v_bitop3_b32 %[u], v217, v219, %[tag] bitop3:0x7e\n"
      "v_or3_b32 %[a], %[a], %[t], %[u]\n"
      "v_bitop3_b32 %[t], v221, v223, %[tag] bitop3:0x7e\n"
      "v_bitop3_b32 %[u], v225, v227, %[tag] bitop3:0x7e\n"
      "v_or3_b32 %[a], %[a], %[t], %[u]\n"
      "v_bitop3_b32 %[t], v229, v231, %[tag] bitop3:0x7e\n"
      "v_bitop3_b32 %[u], v233, v235, %[tag] bitop3:0x7e\n"
      "v_or3_b32 %[a], %[a], %[t], %[u]\n"
      "v_bitop3_b32 %[t], v237, v239, %[tag] bitop3:0x7e\n"
      "v_bitop3_b32 %[u], v241, v243, %[tag] bitop3:0x7e\n"
      "v_or3_b32 %[a], %[a], %[t], %[u]\n"
      "v_bitop3_b32 %[t], v245, v247, %[tag] bitop3:0x7e\n"
      "v_bitop3_b32 %[u], v249, v251, %[tag] bitop3:0x7e\n"
      "v_or3_b32 %[a], %[a], %[t], %[u]\n"
      "v_bitop3_b32 %[t], v253, v255, %[tag] bitop3:0x7e\n"
      "v_or_b32 %[a], %[a], %[t]\n"
      "v_cmp_ne_u32 vcc, 0, %[a]\n"
      "s_cbranch_vccz .Lg_done%=\n"
      "s_sub_u32 %[cnt], %[cnt], 1\n"
      "s_cbranch_scc1 .Lg_done%=\n"
      "s_sleep %[sl]\n"
      "v_bitop3_b32 %[t], v209, v211, %[tag] bitop3:0x7e\n"
      "v_cmp_ne_u32 vcc, 0, %[t]\n"
      "s_mov_b64 exec, vcc\n"
      "buffer_load_dwordx4 v[208:211], %[vo], %[rs], %[s0] offen offset:0 sc1\n"
      "s_mov_b64 exec, -1\n"
      "v_bitop3_b32 %[t], v213, v215, %[tag] bitop3:0x7e\n"
      "v_cmp_ne_u32 vcc, 0, %[t]\n"
      "s_mov_b64 exec, vcc\n"
      "buffer_load_dwordx4 v[212:215], %[vo], %[rs], %[s0] offen offset:1024 sc1\n"
      "s_mov_b64 exec, -1\n"
      "v_bitop3_b32 %[t], v217, v219, %[tag] bitop3:0x7e\n"
      "v_cmp_ne_u32 vcc, 0, %[t]\n"
      "s_mov_b64 exec, vcc\n"
      "buffer_load_dwordx4 v[216:219], %[vo], %[rs], %[s0] offen offset:2048 sc1\n"
      "s_mov_b64 exec, -1\n"
      "v_bitop3_b32 %[t], v221, v223, %[tag] bitop3:0x7e\n"
      "v_cmp_ne_u32 vcc, 0, %[t]\n"
      "s_mov_b64 exec, vcc\n"
      "buffer_load_dwordx4 v[220:223], %[vo], %[rs], %[s0] offen offset:3072 sc1\n"
      "s_mov_b64 exec, -1\n"
      "v_bitop3_b32 %[t], v225, v227, %[tag] bitop3:0x7e\n"
      "v_cmp_ne_u32 vcc, 0, %[t]\n"
      "s_mov_b64 exec, vcc\n"
      "buffer_load_dwordx4 v[224:227], %[vo], %[rs], %[s1] offen offset:0 sc1\n"
      "s_mov_b64 exec, -1\n"
      "v_bitop3_b32 %[t], v229, v231, %[tag] bitop3:0x7e\n"
      "v_cmp_ne_u32 vcc, 0, %[t]\n"
      "s_mov_b64 exec, vcc\n"
      "buffer_load_dwordx4 v[228:231], %[vo], %[rs], %[s1] offen offset:1024 sc1\n"
      "s_mov_b64 exec, -1\n"
      "v_bitop3_b32 %[t], v233, v235, %[tag] bitop3:0x7e\n"
      "v_cmp_ne_u32 vcc, 0, %[t]\n"
      "s_mov_b64 exec, vcc\n"
      "buffer_load_dwordx4 v[232:235], %[vo], %[rs], %[s1] offen offset:2048 sc1\n"
      "s_mov_b64 exec, -1\n"
      "v_bitop3_b32 %[t], v237, v239, %[tag] bitop3:0x7e\n"
      "v_cmp_ne_u32 vcc, 0, %[t]\n"
      "s_mov_b64 exec, vcc\n"
      "buffer_load_dwordx4 v[236:239], %[vo], %[rs], %[s1] offen offset:3072 sc1\n"
      "s_mov_b64 exec, -1\n"
      "v_bitop3_b32 %[t], v241, v243, %[tag] bitop3:0x7e\n"
      "v_cmp_ne_u32 vcc, 0, %[t]\n"
      "s_mov_b64 exec, vcc\n"
      "buffer_load_dwordx4 v[240:243], %[vo], %[rs], %[s2] offen offset:0 sc1\n"
      "s_mov_b64 exec, -1\n"
      "v_bitop3_b32 %[t], v245, v247, %[tag] bitop3:0x7e\n"
      "v_cmp_ne_u32 vcc, 0, %[t]\n"
      "s_mov_b64 exec, vcc\n"
      "buffer_load_dwordx4 v[244:247], %[vo], %[rs], %[s2] offen offset:1024 sc1\n"
      "s_mov_b64 exec, -1\n"
      "v_bitop3_b32 %[t], v249, v251, %[tag] bitop3:0x7e\n"
      "v_cmp_ne_u32 vcc, 0, %[t]\n"
      "s_mov_b64 exec, vcc\n"
      "buffer_load_dwordx4 v[248:251], %[vo], %[rs], %[s2] offen offset:2048 sc1\n"
      "s_mov_b64 exec, -1\n"
      "v_bitop3_b32 %[t], v253, v255, %[tag] bitop3:0x7e\n"
      "v_cmp_ne_u32 vcc, 0, %[t]\n"
      "s_mov_b64 exec, vcc\n"
      "buffer_load_dwordx4 v[252:255], %[vo], %[rs], %[s2] offen offset:3072 sc1\n"
      "s_mov_b64 exec, -1\n"
      "s_branch .Lg_loop%=\n"
      ".Lg_done%=:\n"
      : "=&{v[208:211]}"(v[0]), "=&{v[212:215]}"(v[1]), "=&{v[216:219]}"(v[2]), "=&{v[220:223]}"(v[3]), "=&{v[224:227]}"(v[4]), "=&{v[228:231]}"(v[5]), "=&{v[232:235]}"(v[6]), "=&{v[236:239]}"(v[7]), "=&{v[240:243]}"(v[8]), "=&{v[244:247]}"(v[9]), "=&{v[248:251]}"(v[10]), "=&{v[252:255]}"(v[11]), [a] "=&v"(a), [t] "=&v"(t), [u] "=&v"(u), [cnt] "=&s"(cnt)
      : [vo] "v"(voff), [rs] "s"(r), [tag] "s"(tag), [s0] "s"(soff + 0), [s1] "s"(soff + 4096), [s2] "s"(soff + 8192), [cap] "i"(SPIN_CAP), [sl] "i"(SLEEP) : "vcc", "scc", "memory");
}
DI unsigned now() { return (unsigned)__builtin_amdgcn_s_memrealtime(); }
DI void pace(unsigned t0, unsigned dt) { while (now() - t0 < dt) __builtin_amdgcn_s_sleep(1); }
template <int KS> DI void out_gemv(const v4u* wf, lds_t ystage, rsrc_t ws, int dst, int slice_stride, unsigned tag, int lane) {
  const int c = lane & 15, g = lane >> 4;
  v4u a[KS];
#pragma unroll
  for (int t = 0; t < KS; ++t) a[t] = lds128(ystage + ((c >> 2) & 1) * (64 * KS) + 64 * t + 16 * g);
  f32x4 d[8];
#pragma unroll
  for (int i = 0; i < 8; ++i) d[i] = f32x4{0.f, 0.f, 0.f, 0.f};
#pragma unroll
  for (int t = 0; t < KS; ++t)
#pragma unroll
    for (int i = 0; i < 8; ++i) d[i] = mfma16(a[t], wf[8 * t + i], d[i]);
  const bool hi = lane >= 32;
  float v[4];
#pragma unroll
  for (int i = 0; i < 4; ++i) v[i] = hi ? d[4 + i][0] : d[i][0];
  const int voff = (lane >> 3) * slice_stride + (lane & 7) * 16;
  st_sc(v4u{fu(v[0]), tag, fu(v[1]), tag}, ws, voff, dst);
  st_sc(v4u{fu(v[2]), tag, fu(v[3]), tag}, ws, voff, dst + 8 * slice_stride);
}
template <int NL> DI void final_reduce(rsrc_t ws, int src, int slice, void* out, int colbase, unsigned tag, int lane) {
  v4u v[NL];
  if (NL == 8 && D_FA) __builtin_amdgcn_s_sleep(D_FA);
  gather<NL, NL == 8 ? SL_FA : 0>(v, ws, lane * 16, src, tag);
  float s0 = 0.f, s1 = 0.f;
#pragma unroll
  for (int i = 0; i < NL; ++i) { s0 += uf(v[i][0]); s1 += uf(v[i][2]); }
  s0 += dpp<ROR8>(s0); s1 += dpp<ROR8>(s1);
  s0 = x32_sum(x16_sum(s0)); s1 = x32_sum(x16_sum(s1));
  if (lane < 8) {
    const int l = 8 * (slice & 7) + lane, c = l & 15, g = l >> 4;
    const int col = 128 * (slice >> 4) + 8 * c + 4 * (g >> 1) + 2 * ((slice >> 3) & 1);
    __builtin_amdgcn_raw_buffer_store_b32(pk(s0, s1), mk(out, 4096), ((g & 1) * 1024 + colbase + col) * 2, 0, AUX_SC1);
  }
}

DI void role_a(const Params& p, int x, int i, int lane, int w) {
  STAMP_DECL
  STAMP(0);
  const unsigned t0 = now();
  const int b = x & 1, sp = (x >> 1) * 8 + i, h = 4 * ((i & 6) + (x & 1)) + (((x >> 1) + (i >> 1)) & 3), dh = i & 1;
  const int c = lane & 15, g = lane >> 4;
  const rsrc_t ws = mk(p.ws, WS_BYTES), rc = mk(p.ckv, 2 * 8192 * 1024);
  constexpr int IMG = 32768;
  const int kbase = (b * 8192 + sp * 256) * 1024;
  auto krow = [&](int row) { return kbase + ((row + 32 * i) & 255) * 1024; };
  if (w == 0) dma_sc(ws, 0, WS_EPOCH, 1024);
  {
    const int v0 = (lane ^ fsw(2 * w)) << 4, v1 = (lane ^ fsw(2 * w + 1)) << 4;
#pragma unroll
    for (int j = 0; j < 16; ++j) { const int row = 2 * w + (j & 1) + 16 * (j >> 1); dma_nt(rc, (j & 1) ? v1 : v0, krow(row), IMG + row * 1024); }
  }
  int ts = c * 1024 + 256 * g + fsw(c) * 16;
  const int hh = lane >> 5, blk = (lane >> 4) & 1, q4 = (lane & 15) >> 2, p4 = lane & 3;
  int ta[2][2];
#pragma unroll
  for (int ct = 0; ct < 2; ++ct)
#pragma unroll
    for (int t = 0; t < 2; ++t) { const int row = 8 * hh + 4 * t + q4; ta[ct][t] = IMG + row * 1024 + (((8 * w + 4 * ct + 2 * blk + (p4 >> 1)) ^ fsw(row)) << 4) + 8 * (p4 & 1); PIN(ta[ct][t]); }
  auto trfrag = [&](int ct, int ks) { const v2u r0 = ldstr(ta[ct][0] + ks * 16384), r1 = ldstr(ta[ct][1] + ks * 16384); return v4u{r0[0], r0[1], r1[0], r1[1]}; };
  int vv[4];
#pragma unroll
  for (int j = 0; j < 4; ++j) vv[j] = (lane ^ fsw(4 * (w & 3) + j)) << 4;
  v4u sa[16], pa[8][2];
  VMCNT(8);
  bar();
  STAMP(1);
  const unsigned tag = __builtin_amdgcn_readfirstlane(*LP(unsigned, 1024)) + 1u;
  if (w < 4) {
#pragma unroll
    for (int s = 0; s < 16; ++s) sa[s] = lds128(IMG + w * 16384 + (ts ^ (s << 4)));
#pragma unroll
    for (int s = 0; s < 16; ++s) PIN(sa[s]);
  }
#pragma unroll
  for (int ks = 0; ks < 4; ++ks) { pa[ks][0] = trfrag(0, ks); pa[ks][1] = trfrag(1, ks); }
#pragma unroll
  for (int ks = 0; ks < 4; ++ks) { PIN(pa[ks][0]); PIN(pa[ks][1]); }
  bar();
  if (w >= 4) {
#pragma unroll
    for (int j = 0; j < 16; ++j) { const int row = 4 * (w - 4) + (j & 3) + 16 * (j >> 2); dma_nt(rc, vv[j & 3], krow(128 + row), IMG + row * 1024); }
    VMCNT(16);
  } else {
    VMCNT(0);
  }
  bar();
  STAMP(2);
  PIN(ts);
  if (w >= 4) {
#pragma unroll
    for (int s = 0; s < 16; ++s) sa[s] = lds128(IMG + w * 16384 + (ts ^ (s << 4)));
#pragma unroll
    for (int s = 0; s < 16; ++s) PIN(sa[s]);
  }
#pragma unroll
  for (int ks = 4; ks < 8; ++ks) { pa[ks][0] = trfrag(0, ks); pa[ks][1] = trfrag(1, ks); }
#pragma unroll
  for (int ks = 4; ks < 8; ++ks) { PIN(pa[ks][0]); PIN(pa[ks][1]); }
  bar();
  STAMP(13);
  if (w >= 4) {
#pragma unroll
    for (int j = 16; j < 32; ++j) { const int row = 4 * (w - 4) + (j & 3) + 16 * (j >> 2); vmcnt<WBETA - 1>(); dma_nt(rc, vv[j & 3], krow(128 + row), IMG + row * 1024); }
    STAMP(3);
  } else if (w == 0) {
    for (int k = 0; k < QDELAY; ++k) __builtin_amdgcn_s_sleep(8);
    for (unsigned spins = 0;; ++spins) {
      const v4u f = ld_sc(ws, (lane >> 1) * FSTR + (lane & 1) * 16, WS_QFLAG);
      if (all_ok(f[0] == tag && f[1] == ~tag && f[2] == tag && f[3] == ~tag) || spins >= SPIN_CAP) break;
      asm volatile("" ::: "memory");
      __builtin_amdgcn_s_sleep(2);
    }
    STAMP(3);
#pragma unroll
    for (int j = 0; j < 32; ++j) dma_sc(ws, (lane ^ fsw(j)) << 4, WS_QABS + b * 32768 + j * 1024, j * 1024);
  }
  VMCNT(0);
  bar();
  STAMP(4);
  PIN(ts);
  f32x4 d[2][2];
#pragma unroll
  for (int k = 0; k < 4; ++k) d[k >> 1][k & 1] = f32x4{0.f, 0.f, 0.f, 0.f};
  v4u wv[8], wo[16];
  const rsrc_t rv = mk(p.wuv, 512 * 8192);
  const int vo = (lane >> 3) * 8 * 8192 + (lane & 7) * 16;
  const rsrc_t ro = mk(p.woa, 4096 * 1024);
  auto ld_wo = [&](int n) { return ld_nt(ro, g * 8192 + c * 16, (h * 128 + dh * 64 + 32 * (n >> 3) + (n & 7)) * 1024 + w * 256); };
  {
    v4u q0[3], q1[3], sb[3];
#pragma unroll
    for (int s = 0; s < 18; ++s) {
      if (s < 16) { const int a = ts ^ (s << 4); q0[s % 3] = lds128(a); q1[s % 3] = lds128(a + 16384); sb[s % 3] = lds128(a + (IMG + w * 16384)); }
      SB();
      if (s >= 2) {
        const int t = s - 2;
        d[0][0] = mfma16(sa[t], q0[t % 3], d[0][0]); d[0][1] = mfma16(sa[t], q1[t % 3], d[0][1]);
        d[1][0] = mfma16(sb[t % 3], q0[t % 3], d[1][0]); d[1][1] = mfma16(sb[t % 3], q1[t % 3], d[1][1]);
        { const int k = t < 3 ? t : (t % 3 == 2 ? 2 + t / 3 : -1);
          if (k >= 0) wv[k] = ld_nt(rv, vo, (64 * w + k) * 8192 + h * 256 + dh * 128); }
      }
      SB();
    }
  }
  float m[2];
#pragma unroll
  for (int hg = 0; hg < 2; ++hg) {
    float t = fmaxf(fmaxf(fmaxf(d[0][hg][0], d[0][hg][1]), fmaxf(d[0][hg][2], d[0][hg][3])), fmaxf(fmaxf(d[1][hg][0], d[1][hg][1]), fmaxf(d[1][hg][2], d[1][hg][3])));
    m[hg] = x32_max(x16_max(t));
  }
  constexpr int PST = 528, MX = 17408, LS = 18432, WT = 19456;
  bar();
  STAMP(5);
  if (g < 2) *LP(float, MX + ((16 * g + c) * 8 + w) * 4) = g ? m[1] : m[0];
  bar();
  if ((PRE & 4) && w == 6 && !(i & 1)) dma_sc(ws, lane * 16, WS_AML + (b * 32 + sp) * 512, 24576);
  if (PRE & 8) dma_sc(ws, lane * 16, WS_OPA + (x * 8 + i) * 8192 + w * 1024, 24576);
  wv[7] = ld_nt(rv, vo, (64 * w + 7) * 8192 + h * 256 + dh * 128);
#pragma unroll
  for (int hg = 0; hg < 2; ++hg) {
    const f32x4 a0 = *LP(f32x4, MX + (16 * hg + c) * 32), a1 = *LP(f32x4, MX + (16 * hg + c) * 32 + 16);
    m[hg] = fmaxf(fmaxf(fmaxf(a0[0], a0[1]), fmaxf(a0[2], a0[3])), fmaxf(fmaxf(a1[0], a1[1]), fmaxf(a1[2], a1[3])));
  }
  float l[2];
#pragma unroll
  for (int hg = 0; hg < 2; ++hg) {
    const float mc = -m[hg] * C2;
    float s = 0.f;
#pragma unroll
    for (int ab = 0; ab < 2; ++ab) {
      float e[4];
#pragma unroll
      for (int r = 0; r < 4; ++r) { e[r] = ex2(fmaf(d[ab][hg][r], C2, mc)); s += e[r]; }
      *LP(v2u, (16 * hg + c) * PST + (128 * ab + 16 * w + 4 * g) * 2) = v2u{pk(e[0], e[1]), pk(e[2], e[3])};
    }
    l[hg] = x32_sum(x16_sum(s));
  }
  if (g < 2) *LP(float, LS + ((16 * g + c) * 8 + w) * 4) = g ? l[1] : l[0];
  const float Mh = (g & 1) ? m[1] : m[0];
  bar();
  STAMP(6);
  f32x16 o[2];
#pragma unroll
  for (int k = 0; k < 16; ++k) { o[0][k] = 0.f; o[1][k] = 0.f; }
  {
    int pb = (lane & 31) * PST + 16 * hh;
    PIN(pb); PIN(ta[0][0]); PIN(ta[0][1]); PIN(ta[1][0]); PIN(ta[1][1]);
    v4u pf[3], tb[3][2];
#pragma unroll
    for (int s = 0; s < 18; ++s) {
      if (s < 16) { pf[s % 3] = lds128(pb + 32 * s); if (s >= 8) { tb[s % 3][0] = trfrag(0, s - 8); tb[s % 3][1] = trfrag(1, s - 8); } }
      SB();
      if (s >= 2) {
        const int t = s - 2;
        o[0] = mfma32(t < 8 ? pa[t][0] : tb[t % 3][0], pf[t % 3], o[0]);
        o[1] = mfma32(t < 8 ? pa[t][1] : tb[t % 3][1], pf[t % 3], o[1]);
        if (w < 4 && !(t & 1) && (t >> 1) < WO_EARLY) wo[t >> 1] = ld_wo(t >> 1);
      }
      SB();
    }
  }
  float Lh;
  { const f32x4 a0 = *LP(f32x4, LS + (lane & 31) * 32), a1 = *LP(f32x4, LS + (lane & 31) * 32 + 16); Lh = ((a0[0] + a0[1]) + (a0[2] + a0[3])) + ((a1[0] + a1[1]) + (a1[2] + a1[3])); }
  bar();
  STAMP(7);
  {
    const lds_t stg = IMG + w * 4608;
#pragma unroll
    for (int ct = 0; ct < 2; ++ct)
#pragma unroll
      for (int rq = 0; rq < 4; ++rq)
        *LP(v2u, stg + (lane & 31) * 144 + (32 * ct + 8 * rq + 4 * hh) * 2) = v2u{pk(o[ct][4 * rq], o[ct][4 * rq + 1]), pk(o[ct][4 * rq + 2], o[ct][4 * rq + 3])};
    asm volatile("" ::: "memory");
    v4u v[4];
#pragma unroll
    for (int e = 0; e < 4; ++e) v[e] = lds128(stg + (8 * e + (lane >> 3)) * 144 + (lane & 7) * 16);
    SB();
#pragma unroll
    for (int e = 0; e < 4; ++e) {
      st_sc(v[e], ws, (lane >> 3) * 65536 + (lane & 7) * 16, WS_AO + e * 8 * 65536 + (b * 32 + sp) * 1024 + w * 128);
    }
  }
  VMCNT(0);
  bar();
  STAMP(8);
  if (w == 7 && lane < 32) st_sc(v4u{fu(Mh), tag, fu(Lh), tag}, ws, lane * 16, WS_AML + (b * 32 + sp) * 512);
  if (!TWA && w < 4) {
#pragma unroll
    for (int n = WO_EARLY; n < 16; ++n) wo[n] = ld_wo(n);
  }
  SB();
  transpose8(wv);
#pragma unroll
  for (int n = 0; n < 8; ++n) PIN(wv[n]);
  constexpr int XS = IMG + 40960, G1 = XS + 8448, YS = G1 + 8192;
  {
    v4u f;
    if (D_AML) __builtin_amdgcn_s_sleep(D_AML);
    gather<1, SL_AML>(&f, ws, (lane & 7) * 512, WS_AML + w * 4096 + h * 16, tag);
    STAMP(9);
    v4u t[8];
#pragma unroll
    for (int k = 0; k < 8; ++k) t[k] = ld_sc(ws, lane * 16 + (k & 3) * 1024, WS_AO + h * 65536 + w * 8192 + (k >> 2) * 4096);
    SB();
    const float mm = uf(f[0]);
    float M = fmaxf(mm, dpp<XOR1>(mm)); M = fmaxf(M, dpp<XOR2>(M)); M = fmaxf(M, dpp<ROR4>(M));
    const float e = ex2((mm - M) * C2);
    float Lw = e * uf(f[2]); Lw += dpp<XOR1>(Lw); Lw += dpp<XOR2>(Lw); Lw += dpp<ROR4>(Lw);
    if (lane == 0) *LP(f32x2, WT + w * 8) = f32x2{M, Lw};
    if (!TWA && w < 4) { transpose8(wo); transpose8(wo + 8); }
    float xs[8];
#pragma unroll
    for (int j = 0; j < 8; ++j) xs[j] = 0.f;
#pragma unroll
    for (int k = 0; k < 8; ++k) {
      const float wt = uf(__builtin_amdgcn_readlane(fu(e), k));
#pragma unroll
      for (int j = 0; j < 4; ++j) { xs[2 * j] = fmaf(wt, bflo(t[k][j]), xs[2 * j]); xs[2 * j + 1] = fmaf(wt, bfhi(t[k][j]), xs[2 * j + 1]); }
    }
    *LP(v4u, XS + w * 1056 + lane * 16) = v4u{pk(xs[0], xs[1]), pk(xs[2], xs[3]), pk(xs[4], xs[5]), pk(xs[6], xs[7])};
  }
  bar();
  STAMP(10);
  if (TWA && w < 4) {
    pace(t0, TWA);
#pragma unroll
    for (int n = WO_EARLY; n < 16; ++n) wo[n] = ld_wo(n);
    SB();
  }
  {
    const v4u xa = lds128(XS + (4 * (c >> 3) + (c & 3)) * 1056 + (8 * w + 2 * g + ((c >> 2) & 1)) * 16);
    const f32x4 m0 = *LP(f32x4, WT + hh * 32), m1 = *LP(f32x4, WT + hh * 32 + 16);
    const float M = fmaxf(fmaxf(m0[0], m0[2]), fmaxf(m1[0], m1[2]));
    f32x4 fs = {ex2((m0[0] - M) * C2), ex2((m0[2] - M) * C2), ex2((m1[0] - M) * C2), ex2((m1[2] - M) * C2)};
    fs *= 1.0f / (fs[0] * m0[1] + fs[1] * m0[3] + fs[2] * m1[1] + fs[3] * m1[3]);
    float y[8];
#pragma unroll
    for (int jj = 0; jj < 8; ++jj) { const f32x4 dd = mfma16(xa, wv[jj], f32x4{0.f, 0.f, 0.f, 0.f}); y[jj] = (fs[0] * dd[0] + fs[1] * dd[1]) + (fs[2] * dd[2] + fs[3] * dd[3]); }
    if (((lane >> 3) & 1) == (g & 1)) {
      const lds_t dst = G1 + ((2 * w + (g & 1)) * 128 + (lane >> 5) * 64 + 8 * (lane & 7)) * 4;
      *LP(f32x4, dst) = f32x4{y[0], y[1], y[2], y[3]};
      *LP(f32x4, dst + 16) = f32x4{y[4], y[5], y[6], y[7]};
    }
  }
  bar();
  STAMP(11);
  if (w < 4) {
    f32x2 s = {0.f, 0.f};
#pragma unroll
    for (int k = 0; k < 16; ++k) s += *LP(f32x2, G1 + k * 512 + lane * 8);
    const lds_t ys = YS + w * 256;
    *LP(unsigned, ys + lane * 4) = pk(s[0], s[1]);
    asm volatile("" ::: "memory");
    if (TWA) { transpose8(wo); transpose8(wo + 8); }
    out_gemv<2>(wo, ys, ws, WS_OPA + w * 16 * 8192 + (2 * h + dh) * 128, 8192, tag, lane);
  } else if (w == 4) {
    const int slice = x * 8 + i;
    final_reduce<8>(ws, WS_OPA + slice * 8192, slice, p.out, 0, tag, lane);
  } else if (w == 5 && x == 0 && i == 0) {
    for (unsigned spins = 0;; ++spins) {
      const v4u f = ld_sc(ws, lane * 256, WS_QB);
      if (all_ok(tags_ok(f, tag)) || spins >= SPIN_CAP) break;
      asm volatile("" ::: "memory");
      __builtin_amdgcn_s_sleep(1);
    }
    if (lane == 0) __builtin_amdgcn_raw_buffer_store_b32(tag, ws, 0, WS_EPOCH, AUX_SC1);
  }
  STAMP(12);
  STAMP_FLUSH();
}

constexpr int B_X = 65536, B_QB = 67584, B_QA = 68608, B_ML = 69120, B_E = 69376, B_CNT = 69632, B_WT = 69888, B_EP = 70656, B_KS = 73728;
#define ST_ARG
#define ST_PASS

DI void k_write(const v4u& kr, int j, lds_t my, int c, int g) { const int key = 4 * j + g; *LP(v4u, my + key * 256 + ((c ^ (key & 15)) << 4)) = kr; }
DI void k_read(v4u* kr, lds_t my, int c, int g) {
  const int k0 = 4 * (c & 3) + (c >> 2);
#pragma unroll
  for (int T = 0; T < 2; ++T)
#pragma unroll
    for (int s = 0; s < 4; ++s) kr[4 * T + s] = lds128(my + (16 * T + k0) * 256 + (((4 * s + g) ^ k0) << 4));
}
DI unsigned lds_arrive(lds_t cnt, int lane) {
  unsigned old = 0;
  asm volatile("s_waitcnt lgkmcnt(0)" ::: "memory");
  if (lane == 0) old = __hip_atomic_fetch_add(LP(unsigned, cnt), 1u, __ATOMIC_RELAXED, __HIP_MEMORY_SCOPE_WORKGROUP);
  old = __builtin_amdgcn_readfirstlane(old);
  asm volatile("" ::: "memory");
  return old;
}
template <int NU> DI void b_scores(const v4u* kr, v4u* pf, rsrc_t ws, int w, int lane, int nwaves, int mlbase, unsigned tag) {
  const int c = lane & 15, g = lane >> 4;
  v4u qf[4];
#pragma unroll
  for (int s = 0; s < 4; ++s) qf[s] = lds128(B_QB + (c >> 2) * 256 + 64 * s + 16 * g);
  float sc[8 * NU];
#pragma unroll
  for (int T = 0; T < 2 * NU; ++T) {
    f32x4 d = {0.f, 0.f, 0.f, 0.f};
#pragma unroll
    for (int s = 0; s < 4; ++s) d = mfma16(kr[4 * T + s], qf[s], d);
#pragma unroll
    for (int r = 0; r < 4; ++r) sc[4 * T + r] = d[r];
  }
  float m = sc[0];
#pragma unroll
  for (int k = 1; k < 8 * NU; ++k) m = fmaxf(m, sc[k]);
  m = x32_max(x16_max(m));
  float l = 0.f;
  const float mc = -m * C2;
#pragma unroll
  for (int k = 0; k < 8 * NU; ++k) { sc[k] = ex2(fmaf(sc[k], C2, mc)); l += sc[k]; }
  l = x32_sum(x16_sum(l));
#pragma unroll
  for (int u = 0; u < NU; ++u)
#pragma unroll
    for (int t = 0; t < 4; ++t) pf[u][t] = pk(sc[8 * u + 2 * t], sc[8 * u + 2 * t + 1]);
  if (lane < 16 && (c & 3) == 0) *LP(f32x2, B_ML + (w * 4 + (c >> 2)) * 8) = f32x2{m, l};
  if ((int)lds_arrive(B_CNT, lane) == nwaves - 1) {
    const f32x2 ml = *LP(f32x2, B_ML + (lane & 31) * 8);
    float M = ml[0];
    M = fmaxf(M, dpp<ROR4>(M)); M = fmaxf(M, dpp<ROR8>(M)); M = x16_max(M);
    const float e = ex2((ml[0] - M) * C2);
    float t = ml[1] * e;
    t += dpp<ROR4>(t); t += dpp<ROR8>(t); t = x16_sum(t);
    if (lane < 32) *LP(float, B_E + ((lane & 3) * 8 + (lane >> 2)) * 4) = e;
    if (lane < 8) st_sc(v4u{fu(M), tag, fu(t), tag}, ws, lane * 16, mlbase);
  }
}
DI void b_pv(const v4u& pf, const v4u* vr, f32x4* acc) {
  v4u t[8];
#pragma unroll
  for (int j = 0; j < 4; ++j) {
#pragma unroll
    for (int k = 0; k < 8; ++k) t[k][j] = __builtin_amdgcn_perm(vr[2 * j + 1][k >> 1], vr[2 * j][k >> 1], (k & 1) ? 0x07060302u : 0x05040100u);
    SB();
  }
#pragma unroll
  for (int k = 0; k < 8; ++k) acc[k] = mfma16(pf, t[k], acc[k]);
#pragma unroll
  for (int k = 0; k < 8; ++k) PIN(acc[k]);
}
DI void b_merge(const f32x4* acc, rsrc_t ws, int w, int lane, int nwaves, int obase, unsigned tag ST_ARG) {
  const lds_t mo = B_KS + w * 8192 + lane * 32;
  *LP(f32x4, mo) = f32x4{acc[0][0], acc[1][0], acc[2][0], acc[3][0]};
  *LP(f32x4, mo + 16) = f32x4{acc[4][0], acc[5][0], acc[6][0], acc[7][0]};
  if ((int)lds_arrive(B_CNT + 4, lane) == nwaves - 1) {
    int ob = B_KS + lane * 16, eb = B_E + (lane >> 5) * 32;
    PIN(ob); PIN(eb);
    f32x4 e[2][2], v[2][8];
#pragma unroll
    for (int eh = 0; eh < 2; ++eh) {
      e[eh][0] = *LP(f32x4, eb + eh * 64); e[eh][1] = *LP(f32x4, eb + eh * 64 + 16);
#pragma unroll
      for (int k = 0; k < 8; ++k) v[eh][k] = *LP(f32x4, ob + k * 8192 + eh * 1024);
    }
    SB();
#pragma unroll
    for (int eh = 0; eh < 2; ++eh) {
      f32x4 s = e[eh][0][0] * v[eh][0];
#pragma unroll
      for (int k = 1; k < 8; ++k) s += e[eh][k >> 2][k & 3] * v[eh][k];
      st_sc(v4u{pk(s[0], s[1]), tag, pk(s[2], s[3]), tag}, ws, (lane >> 5) * 12288 + (lane & 31) * 16, obase + eh * 2 * 12288);
    }
    STAMP(8);
  }
}
DI void lds_signal(lds_t cnt, int lane) { if (lane == 0) (void)__hip_atomic_fetch_add(LP(unsigned, cnt), 1u, __ATOMIC_RELAXED, __HIP_MEMORY_SCOPE_WORKGROUP); }
DI void lds_wait(lds_t cnt, unsigned want) {
  for (unsigned spins = 0; *(volatile LAS unsigned*)(size_t)cnt < want && spins < LSPIN; ++spins) __builtin_amdgcn_s_sleep(1);
  asm volatile("" ::: "memory");
}
template <int NCT> DI void gemv_n(f32x4* d, int ct0, lds_t wb, lds_t xb, int c, int g) {
  const int q4 = c >> 2, p4 = c & 3, fx = (q4 >> 1) | ((g & 1) << 1);
  int a[NCT];
#pragma unroll
  for (int ct = 0; ct < NCT; ++ct) { a[ct] = wb + (8 * g + q4) * 128 + (((ct0 + ct) ^ fx) << 5) + 8 * p4; PIN(a[ct]); }
  int xa = xb + (c & 1) * 512 + 16 * g;
  PIN(xa);
  constexpr int D = NCT == 1 ? 4 : 1;
  v4u xf[D + 1], wf[D + 1][NCT];
#pragma unroll
  for (int s = 0; s < 8 + D; ++s) {
    if (s < 8) {
      xf[s % (D + 1)] = lds128(xa + 64 * s);
#pragma unroll
      for (int ct = 0; ct < NCT; ++ct) { const v2u r0 = ldstr(a[ct] + 4096 * s), r1 = ldstr(a[ct] + 4096 * s + 512); wf[s % (D + 1)][ct] = v4u{r0[0], r0[1], r1[0], r1[1]}; }
    }
    SB();
    if (s >= D) {
#pragma unroll
      for (int ct = 0; ct < NCT; ++ct) d[ct] = mfma16(wf[(s - D) % (D + 1)][ct], xf[(s - D) % (D + 1)], d[ct]);
    }
    SB();
  }
}

template <int NU, int LS, int WIN>
DI void b_stream(unsigned t0, rsrc_t rk, rsrc_t rv, rsrc_t ws, int ub, int w, int lane, int nwaves, int obase, int mlbase ST_ARG) {
  const int c = lane & 15, g = lane >> 4;
  const lds_t my = B_KS + w * 8192;
  constexpr int NL = 8 * NU;
  auto off = [&](int n) { return NU == 1 ? ub + n * LS : ub + (n / GI) * (12 * GI * LS) + (n % GI) * LS; };
  v4u kr[NL], vr[NL], pf[NU];
  f32x4 acc[8];
#pragma unroll
  for (int k = 0; k < 8; ++k) acc[k] = f32x4{0.f, 0.f, 0.f, 0.f};
#pragma unroll
  for (int n = 0; n < NL + WQK; ++n) {
    if (n < WIN) vmcnt<WIN - 1>();
    if (PB && NU > 1 && n >= 8 && n % PSTEP == 0) pace(t0, PA + n * PB / 8);
    if (n < NL) kr[n] = ld_nt(rk, lane * 16, off(n));
    else vr[n - NL] = ld_nt(rv, lane * 16, off(n - NL));
    SB();
    const int t = n + 1 - WIN;
    if (t >= 0 && t < NL) { k_write(kr[t], t & 7, my, c, g); if ((t & 7) == 7) k_read(kr + (t - 7), my, c, g); }
    SB();
  }
  STAMP(3);
  lds_wait(B_CNT + 8, 1u);
  const unsigned tag = __builtin_amdgcn_readfirstlane(*LP(unsigned, B_EP)) + 1u;
  STAMP(4);
  b_scores<NU>(kr, pf, ws, w, lane, nwaves, mlbase, tag);
  SB();
  STAMP(5);
#pragma unroll
  for (int n = WQK; n < NL; ++n) {
    vmcnt<WIN - 1>();
    if (PB && n % PSTEP == 0) pace(t0, PA + (NL + n) * PB / 8);
    vr[n] = ld_nt(rv, lane * 16, off(n));
    SB();
    if (n == 13) { b_pv(pf[0], vr, acc); SB(); }
  }
  STAMP(6);
  if (NU > 1) lds_signal(B_CNT + 24, lane);
  if (NU > 1) { b_pv(pf[NU - 2], vr + 8 * (NU - 2), acc); SB(); }
  b_pv(pf[NU - 1], vr + 8 * (NU - 1), acc);
  SB();
  STAMP(7);
  b_merge(acc, ws, w, lane, nwaves, obase, tag ST_PASS);
}
DI void fetch_qb(unsigned t0, rsrc_t ws, int src, unsigned tag, int lane) {
  if (QBT) pace(t0, QBT);
  v4u f[2];
  gather<2, 4>(f, ws, lane * 16, src, tag);
  const v4u f0 = f[0], f1 = f[1];
  *LP(v2u, B_QB + lane * 8) = v2u{f0[0], f0[2]};
  *LP(v2u, B_QB + 512 + lane * 8) = v2u{f1[0], f1[2]};
  asm volatile("s_waitcnt lgkmcnt(0)" ::: "memory");
  if (lane == 0) *(volatile LAS unsigned*)(size_t)(B_CNT + 8) = 1u;
}

DI void role_b(const Params& p, int x, int j, int lane, int w) {
  STAMP_DECL
  STAMP(0);
  const unsigned t0 = now();
  const int type = j % 3, k3 = j / 3, b = j / 12, sp = j % 12;
  const bool tq = type != 2;
  const int c = lane & 15, g = lane >> 4;
  const int col = 8 * k3 + ((x + k3) & 7);
  const rsrc_t ws = mk(p.ws, WS_BYTES);
  const rsrc_t rw = mk(tq ? p.wqa : p.wqb, 512 * 8192);
  const int sb = (type & 1) * 256 * 8192 + col * 128;
  const int vn0 = (lane >> 3) * 8192 + (((lane & 7) ^ ((g & 1) << 1)) << 4), vn1 = vn0 ^ 64;
  auto dma_w = [&](int n) {
    dma_nt(rw, (n & 1) ? vn1 : vn0, sb + ((n >> 2) * 256 + 32 * w + 8 * (n & 3)) * 8192, (n >> 2) * 32768 + (4 * w + (n & 3)) * 1024);
  };
  const int hq = col >> 1, hu = (hq & ~3) | ((hq + 2) & 3), cq = 2 * (col & 1) + type;
  constexpr int DUMMY = 71680;
  if (tq) {
#pragma unroll
    for (int n = 0; n < 4; ++n) dma_w(n);
  } else {
    const int ka = (3 - x - ((unsigned)(size_t)p.wqa >> 7)) & 7, kb = (3 - x - ((unsigned)(size_t)p.wqb >> 7)) & 7;
    const int vp = (lane >> 3) * 8192 + (lane & 7) * 16, sp0 = (8 * k3 + w) * 8 * 8192;
    dma_df(mk(p.wqa, 512 * 8192), vp, sp0 + (8 * ka + ((x + ka) & 7)) * 128, DUMMY);
    dma_df(rw, vp, sp0 + (8 * kb + ((x + kb) & 7)) * 128, DUMMY);
    dma_w(0); dma_w(1);
  }
  if (w == 0) {
    const rsrc_t rx = mk(tq ? p.xa : p.xb, 2048);
    const int vx = (lane >> 5) * 1024 + (lane & 31) * 16;
    dma_df(rx, vx, (type & 1) * 512, B_X); dma_df(rx, vx, 512, B_X + 1024);
    dma_sc(ws, 0, WS_EPOCH, B_EP);
    if ((PRE & 1) && tq) dma_sc(ws, lane * 16, WS_QA + hu * 4096 + cq * 1024, DUMMY);
    if ((PRE & 1) && !tq && k3 == 0) dma_sc(ws, lane * 16, WS_QFLAG, DUMMY);
    if (lane < 8) *LP(unsigned, B_CNT + lane * 4) = 0u;
    if (lane < 4) *LP(f32x2, B_ML + lane * 8) = f32x2{-INFINITY, 0.f};
    *LP(f32x4, B_KS + lane * 32) = f32x4{0.f, 0.f, 0.f, 0.f}; *LP(f32x4, B_KS + lane * 32 + 16) = f32x4{0.f, 0.f, 0.f, 0.f};
  }
  const rsrc_t rk = mk(p.kc, 16 * 8192 * 256), rv = mk(p.vc, 16 * 8192 * 256);
  const int pbase = (b * 8 + x) * 8192 * 256;
  const int obase = WS_BO + 4 * x * 12288 + sp * 1024 + b * 512, mlbase = WS_BML + x * 4096 + b * 2048 + sp * 128;
  if (!tq) {
    bar();
#pragma unroll
    for (int n = 2; n < 8; ++n) { VMCNT(4); dma_w(n); }
    VMCNT(4);
    if (w > 0) {
      lds_signal(B_CNT + 12, lane);
      STAMP(1);
      const rsrc_t rc = mk(p.ckv, 2 * 8192 * 1024);
      const int cb = ((x & 1) * 8192 + (x >> 1) * 2048 + k3 * 256) * 1024;
#pragma unroll
      for (int s = 0; s < PFN; ++s) {
        VMCNT(4);
        if (s == 4) { lds_signal(B_CNT + 16, lane); STAMP(2); }
        dma_df(rc, lane * 16, cb + ((256 - 7 * PFN + 7 * s + w - 1 + 32 * k3) & 255) * 1024, DUMMY);
      }
    }
    STAMP(12);
    {
      int cs = 0;
#pragma unroll
      for (int i = 0; i < 8; ++i) { const int cc = 2 * (4 * ((i & 6) + (x & 1)) + (((x >> 1) + (i >> 1)) & 3)) + (i & 1); if (((cc + ((unsigned)(size_t)p.wuv >> 7)) & 7) == 3) cs = cc; }
      if (w > 0) dma_df(mk(p.wuv, 512 * 8192), (lane >> 3) * 8192 + (lane & 7) * 16, (8 * k3 + w) * 8 * 8192 + cs * 128, DUMMY);
    }
  } else {
    bar();
    const rsrc_t ru = mk(p.wuk, 512 * 8192);
    const int vt = g * 8192 + ((c ^ (g | ((w & 1) << 3))) << 4);
#pragma unroll
    for (int n = 0; n < 4; ++n) { VMCNT(3); dma_nt(ru, vt ^ ((n >> 1) << 6), (cq * 128 + 16 * w + 4 * n) * 8192 + hu * 256, 32768 + (4 * w + n) * 1024); }
    VMCNT(4);
    bar();
    STAMP(1);
    if (w >= 4) {
      const unsigned tag = __builtin_amdgcn_readfirstlane(*LP(unsigned, B_EP)) + 1u;
      f32x4 d = {0.f, 0.f, 0.f, 0.f};
      gemv_n<1>(&d, w - 4, 0, B_X, c, g);
      if (c < 4) st_sc(v4u{fu(c < 2 ? d[0] : d[2]), tag, fu(c < 2 ? d[1] : d[3]), tag}, ws, (c & 1) * 1024 + g * 32 + (c >> 1) * 16, WS_QA + hq * 4096 + type * 2048 + (col & 1) * 512 + (w - 4) * 128);
    }
    VMCNT(0);
    bar();
    STAMP(12);
    if (w > 0) { lds_wait(B_CNT + 20, 1u); STAMP(9); }
  }
  if (w > 0) {
    b_stream<3, 7168, W1>(t0, rk, rv, ws, pbase + sp * GI * 7168 + (w - 1) * 1024, w, lane, tq ? 7 : 8, obase, mlbase ST_PASS);
    STAMP_FLUSH();
    return;
  }
  if (!tq) {
    lds_wait(B_CNT + 12, 7u);
    STAMP(1);
    const unsigned tag = __builtin_amdgcn_readfirstlane(*LP(unsigned, B_EP)) + 1u;
    f32x4 d[4];
#pragma unroll
    for (int ct = 0; ct < 4; ++ct) d[ct] = f32x4{0.f, 0.f, 0.f, 0.f};
#pragma unroll 1
    for (int part = 0; part < 2; ++part) {
      if (part) { VMCNT(0); lds_wait(B_CNT + 16, 7u); STAMP(2); }
      gemv_n<4>(d, 0, part * 32768, B_X + part * 1024, c, g);
    }
    const int ct = (c >> 1) & 3;
    const f32x4 dd = ct == 0 ? d[0] : ct == 1 ? d[1] : ct == 2 ? d[2] : d[3];
    if (c < 8) st_sc(v4u{pk(dd[0], dd[1]), tag, pk(dd[2], dd[3]), tag}, ws, (c & 1) * 16384 + ct * 64 + g * 16, WS_QB + col * 256);
    STAMP(11);
    fetch_qb(t0, ws, WS_QB + (b * 8 + x) * 2048, tag, lane);
    STAMP(13);
    if (PRE & 16) {
#pragma unroll
      for (int n = 0; n < 4; ++n) dma_sc(ws, lane * 16, WS_OPB + (8 * x + k3) * 4096 + n * 1024, DUMMY);
    }
    b_stream<1, 1024, WCTL>(t0, rk, rv, ws, pbase + (252 + (k3 & 3)) * 8192, 0, lane, 8, obase, mlbase ST_PASS);
    lds_wait(B_CNT + 4, 8u);
    STAMP(9);
    const int slice = 8 * x + k3;
    final_reduce<4>(ws, WS_OPB + slice * 4096, slice, p.out, 512, tag, lane);
    STAMP(10);
    STAMP_FLUSH();
    return;
  }
  const unsigned tag = __builtin_amdgcn_readfirstlane(*LP(unsigned, B_EP)) + 1u;
  {
    v4u wf[32];
    {
      const int r0 = 32768 + (8 * (c >> 2) + (c & 3)) * 256;
#pragma unroll
      for (int s = 0; s < 4; ++s) {
        int a = r0 + (((4 * s + g) ^ c) << 4);
        PIN(a);
#pragma unroll
        for (int u = 0; u < 8; ++u) wf[4 * u + s] = lds128(a + (u >> 1) * 8192 + (u & 1) * 1024);
      }
    }
    v4u f[4];
    gather<4, 0>(f, ws, lane * 16, WS_QA + hu * 4096, tag);
    STAMP(2);
    *LP(unsigned, B_QA + lane * 4) = pk(uf(f[0][0]) + uf(f[2][0]), uf(f[0][2]) + uf(f[2][2]));
    *LP(unsigned, B_QA + 256 + lane * 4) = pk(uf(f[1][0]) + uf(f[3][0]), uf(f[1][2]) + uf(f[3][2]));
    v4u qf[4];
#pragma unroll
    for (int s = 0; s < 4; ++s) qf[s] = lds128(B_QA + (c & 1) * 256 + 64 * s + 16 * g);
    f32x4 e[8];
#pragma unroll
    for (int u = 0; u < 8; ++u) e[u] = f32x4{0.f, 0.f, 0.f, 0.f};
#pragma unroll
    for (int s = 0; s < 4; ++s)
#pragma unroll
      for (int u = 0; u < 8; ++u) e[u] = mfma16(wf[4 * u + s], qf[s], e[u]);
    const int ct = (c >> 1) & 3;
    const f32x4 d0 = ct == 0 ? e[0] : ct == 1 ? e[2] : ct == 2 ? e[4] : e[6], d1 = ct == 0 ? e[1] : ct == 1 ? e[3] : ct == 2 ? e[5] : e[7];
    if (c < 8) st_sc(v4u{pk(d0[0], d0[1]), pk(d0[2], d0[3]), pk(d1[0], d1[1]), pk(d1[2], d1[3])}, ws, (c & 1) * 32768 + ct * 64 + g * 16, WS_QABS + hu * 1024 + cq * 256);
    STAMP(3);
    VMCNT(0);
    if (lane == 0) __builtin_amdgcn_raw_buffer_store_b64(v2u{tag, ~tag}, ws, WS_QFLAG + hu * FSTR + cq * 8, 0, AUX_SC1);
    STAMP(4);
    if (lane == 0) *(volatile LAS unsigned*)(size_t)(B_CNT + 20) = 1u;
  }
  for (int k = 0; k < QBDELAY; ++k) __builtin_amdgcn_s_sleep(8);
  STAMP(5);
  fetch_qb(t0, ws, WS_QB + (b * 8 + x) * 2048, tag, lane);
  STAMP(6);
  {
    const int k2 = 2 * k3 + type, h = 4 * x + (k2 >> 2), cg = k2 & 3;
    const rsrc_t ro = mk(p.wob, 4096 * 1024);
    if (PRE & 2) {
#pragma unroll
      for (int n = 0; n < 3; ++n) dma_sc(ws, lane * 16, WS_BO + h * 12288 + (3 * cg + n) * 1024, DUMMY);
    }
    v4u wo[32];
    auto ld_wo = [&](int n) { return ld_nt(ro, g * 8192 + c * 16, (h * 128 + 32 * (n >> 3) + (n & 7)) * 1024 + cg * 256); };
#pragma unroll
    for (int n = 0; n < WO1; ++n) { wo[n] = ld_wo(n); vmcnt<WCTL - 1>(); SB(); }
#pragma unroll
    for (int t = 0; t < WO1 / 8; ++t) transpose8(wo + 8 * t);
#pragma unroll
    for (int n = 0; n < WO1; ++n) PIN(wo[n]);
    STAMP(7);
    lds_wait(B_CNT + 24, WO_LATE);
    float wt[12];
    {
      const bool act = (lane & 31) < 12;
      const int mo = (lane >> 5) * 2048 + (act ? (lane & 31) : 0) * 128;
      v4u f = ld_sc(ws, mo, WS_BML + x * 4096 + (h & 3) * 16);
      SB();
      if (TWO) pace(t0, TWO);
#pragma unroll
      for (int n = WO1; n < 32; ++n) wo[n] = ld_wo(n);
      SB();
      for (unsigned spins = 0; !all_ok(tags_ok(f, tag)) && spins < SPIN_CAP; ++spins) {
        asm volatile("" ::: "memory");
        __builtin_amdgcn_s_sleep(4);
        f = ld_sc(ws, mo, WS_BML + x * 4096 + (h & 3) * 16);
      }
      const float mm = uf(f[0]), M = row_max(mm);
      const float e = act ? ex2((mm - M) * C2) : 0.f, Lt = row_sum(e * uf(f[2]));
      if ((lane & 16) == 0) *LP(float, B_WT + ((lane >> 5) * 16 + c) * 4) = e / Lt;
      asm volatile("" ::: "memory");
#pragma unroll
      for (int s = 0; s < 3; ++s) {
        const f32x4 t = *LP(f32x4, B_WT + (lane >> 5) * 64 + s * 16);
        wt[4 * s] = t[0]; wt[4 * s + 1] = t[1]; wt[4 * s + 2] = t[2]; wt[4 * s + 3] = t[3];
      }
    }
    STAMP(8);
    if (WO1 < 32) {
#pragma unroll
      for (int t = WO1 / 8; t < 4; ++t) transpose8(wo + 8 * t);
#pragma unroll
      for (int n = WO1; n < 32; ++n) PIN(wo[n]);
    }
    STAMP(11);
    lds_wait(B_CNT + 4, 7u);
    v4u v[12];
    gather<12, SL_BO>(v, ws, lane * 16, WS_BO + h * 12288, tag);
    STAMP(13);
    float o[4] = {0.f, 0.f, 0.f, 0.f};
#pragma unroll
    for (int s = 0; s < 12; ++s) {
      o[0] = fmaf(wt[s], bflo(v[s][0]), o[0]); o[1] = fmaf(wt[s], bfhi(v[s][0]), o[1]);
      o[2] = fmaf(wt[s], bflo(v[s][2]), o[2]); o[3] = fmaf(wt[s], bfhi(v[s][2]), o[3]);
    }
    STAMP(9);
    *LP(v2u, B_QA + lane * 8) = v2u{pk(o[0], o[1]), pk(o[2], o[3])};
    asm volatile("" ::: "memory");
    out_gemv<4>(wo, B_QA, ws, WS_OPB + cg * 16 * 4096 + h * 128, 4096, tag, lane);
    STAMP(10);
  }
  STAMP_FLUSH();
}

__global__ void __launch_bounds__(512, 2) k_fused(Params p) {
  const int lane = threadIdx.x & 63, w = __builtin_amdgcn_readfirstlane(threadIdx.x >> 6);
  const int x = blockIdx.x & 7, i = blockIdx.x >> 3;
  if (w >= 6 && i + 32 * (7 - w) < CODE_KB) {
    const rsrc_t rcode = mk((const void*)(__builtin_amdgcn_s_getpc() & ~255ull), CODE_KB * 1024);
    dma_df(rcode, lane * 16, (i + 32 * (7 - w)) * 1024, i >= 24 ? 2048 : 71680);
  }
  if (i >= 24) role_a(p, x, i - 24, lane, w);
  else role_b(p, x, i, lane, w);
}

extern "C" void kernel_launch(void* const* d_in, const int* in_sizes, int n_in, void* d_out, int out_size, void* d_ws, size_t ws_size, hipStream_t stream) {
  Params p;
  p.xa = d_in[0]; p.ckv = d_in[1]; p.wqa = d_in[2]; p.wuk = d_in[3]; p.wuv = d_in[4]; p.woa = d_in[5];
  p.xb = d_in[6]; p.kc = d_in[7]; p.vc = d_in[8]; p.wqb = d_in[9]; p.wob = d_in[10];
  p.out = d_out; p.ws = d_ws;
  (void)hipFuncSetAttribute((const void*)k_fused, hipFuncAttributeMaxDynamicSharedMemorySize, 163840);
  k_fused<<<256, 512, 163840, stream>>>(p);
}
```

```cpp
#define STAMPS 1
#include <hip/hip_runtime.h>
#include <hip/hip_bf16.h>

#define LAS __attribute__((address_space(3)))
#define DI __device__ __forceinline__
typedef unsigned v4u __attribute__((ext_vector_type(4)));
typedef unsigned v2u __attribute__((ext_vector_type(2)));
typedef float f32x4 __attribute__((ext_vector_type(4)));
typedef float f32x2 __attribute__((ext_vector_type(2)));
typedef float f32x16 __attribute__((ext_vector_type(16)));
typedef __bf16 bf16x8 __attribute__((ext_vector_type(8)));
typedef __bf16 bf16x2 __attribute__((ext_vector_type(2)));
typedef short v4s __attribute__((ext_vector_type(4)));
typedef unsigned short u16;
typedef __amdgpu_buffer_rsrc_t rsrc_t;
typedef unsigned lds_t;
#define LP(T, a) ((LAS T*)(size_t)(unsigned)(a))

constexpr int AUX_NT = 2, AUX_SC1 = 16;
constexpr float C2 = 0.088388347648318440f * 1.4426950408889634f;
constexpr unsigned SPIN_CAP = 1u << 19, LSPIN = 1u << 22;

constexpr int WS_EPOCH = 0;
constexpr int WS_QFLAG = 16384;
constexpr int WS_QA = 1572864;
constexpr int WS_QB = 65536;
constexpr int WS_AML = 98304;
constexpr int WS_QABS = 131072;
constexpr int WS_BML = 196608;
constexpr int WS_OPB = 262144;
constexpr int WS_OPA = 524288;
constexpr int WS_BO = 1048576;
constexpr int WS_AO = 2097152;
constexpr int WS_BYTES = 4194304;
constexpr int WS_ST = 4194304;

struct Params {
  const void *xa, *ckv, *wqa, *wuk, *wuv, *woa, *xb, *kc, *vc, *wqb, *wob;
  void *out, *ws;
};

DI rsrc_t mk(const void* p, int bytes) { return __builtin_amdgcn_make_buffer_rsrc((void*)p, (short)0, bytes, 0x00020000); }
DI v4u ld_nt(rsrc_t r, int voff, int soff) { return __builtin_amdgcn_raw_buffer_load_b128(r, voff, soff, AUX_NT); }
DI v4u ld_sc(rsrc_t r, int voff, int soff) { return __builtin_amdgcn_raw_buffer_load_b128(r, voff, soff, AUX_SC1); }
DI void st_sc(v4u v, rsrc_t r, int voff, int soff) { __builtin_amdgcn_raw_buffer_store_b128(v, r, voff + soff, 0, AUX_SC1); }
DI void dma_nt(rsrc_t r, int voff, int soff, unsigned lds) {
  asm volatile("s_mov_b32 m0, %0\n\ts_nop 0\n\tbuffer_load_dwordx4 %1, %2, %3 offen nt lds" ::"s"(lds), "v"(voff), "s"(r), "s"(soff) : "memory");
}
DI void dma_df(rsrc_t r, int voff, int soff, unsigned lds) {
  asm volatile("s_mov_b32 m0, %0\n\ts_nop 0\n\tbuffer_load_dwordx4 %1, %2, %3 offen lds" ::"s"(lds), "v"(voff), "s"(r), "s"(soff) : "memory");
}
DI void dma_sc(rsrc_t r, int voff, int soff, unsigned lds) {
  asm volatile("s_mov_b32 m0, %0\n\ts_nop 0\n\tbuffer_load_dwordx4 %1, %2, %3 offen sc1 lds" ::"s"(lds), "v"(voff), "s"(r), "s"(soff) : "memory");
}
#define VMCNT(n) asm volatile("s_waitcnt vmcnt(" #n ")" ::: "memory")
template <int N> DI void vmcnt() { asm volatile("s_waitcnt vmcnt(%0)" ::"i"(N) : "memory"); }
#define SB() __builtin_amdgcn_sched_barrier(0)
#define PIN(x) asm volatile("" : "+v"(x))
#define W1 6
#define PFN 8
#define PFWV 1
#define WQK 8
#define WCTL 5
#define WBETA 7
#define WO_EARLY 8
#define WV_S 5
#define FSTR 32
#define QDELAY 0
#define ILV 1
#define QBDELAY 0
#define PA 0
#define PB 0
#define PSTEP 4
#define WO1 0
#define WO_LATE 7
#define QBT 700
#define SL_AML 8
#define D_AML 0
#define SL_BO 0
#define SL_FA 0
#define D_FA 30
#define PRE 3
#define GI 2
#define TWO 0
#define TWA 0
#define HOLD 0
#define CODE_KB 33
DI void bar() { asm volatile("s_waitcnt lgkmcnt(0)\n\ts_barrier" ::: "memory"); }
DI float bflo(unsigned u) { return __builtin_bit_cast(float, u << 16); }
DI float bfhi(unsigned u) { return __builtin_bit_cast(float, u & 0xffff0000u); }
DI unsigned fu(float f) { return __builtin_bit_cast(unsigned, f); }
DI float uf(unsigned u) { return __builtin_bit_cast(float, u); }
DI unsigned pk(float lo, float hi) { f32x2 v = {lo, hi}; return __builtin_bit_cast(unsigned, __builtin_convertvector(v, bf16x2)); }
DI float ex2(float x) { return __builtin_amdgcn_exp2f(x); }
DI f32x4 mfma16(v4u a, v4u b, f32x4 c) { return __builtin_amdgcn_mfma_f32_16x16x32_bf16(__builtin_bit_cast(bf16x8, a), __builtin_bit_cast(bf16x8, b), c, 0, 0, 0); }
DI f32x16 mfma32(v4u a, v4u b, f32x16 c) { return __builtin_amdgcn_mfma_f32_32x32x16_bf16(__builtin_bit_cast(bf16x8, a), __builtin_bit_cast(bf16x8, b), c, 0, 0, 0); }
DI v4u lds128(lds_t p) { return *LP(v4u, p); }
DI v2u ldstr(lds_t p) { return __builtin_bit_cast(v2u, __builtin_amdgcn_ds_read_tr16_b64_v4i16(LP(v4s, p))); }
DI float x16_max(float x) { auto s = __builtin_amdgcn_permlane16_swap(fu(x), fu(x), false, false); return fmaxf(uf(s[0]), uf(s[1])); }
DI float x32_max(float x) { auto s = __builtin_amdgcn_permlane32_swap(fu(x), fu(x), false, false); return fmaxf(uf(s[0]), uf(s[1])); }
DI float x16_sum(float x) { auto s = __builtin_amdgcn_permlane16_swap(fu(x), fu(x), false, false); return uf(s[0]) + uf(s[1]); }
DI float x32_sum(float x) { auto s = __builtin_amdgcn_permlane32_swap(fu(x), fu(x), false, false); return uf(s[0]) + uf(s[1]); }
template <int CTRL> DI float dpp(float x) { return uf(__builtin_amdgcn_mov_dpp(fu(x), CTRL, 0xf, 0xf, true)); }
constexpr int XOR1 = 0xB1, XOR2 = 0x4E, ROR4 = 0x124, ROR8 = 0x128;
DI float row_max(float x) { x = fmaxf(x, dpp<XOR1>(x)); x = fmaxf(x, dpp<XOR2>(x)); x = fmaxf(x, dpp<ROR4>(x)); return fmaxf(x, dpp<ROR8>(x)); }
DI float row_sum(float x) { x += dpp<XOR1>(x); x += dpp<XOR2>(x); x += dpp<ROR4>(x); return x + dpp<ROR8>(x); }
DI unsigned fsw(unsigned row) { return ((row & 3) << 2) | ((row >> 2) & 3); }
DI bool tags_ok(v4u v, unsigned tag) { return v[1] == tag && v[3] == tag; }
DI bool all_ok(bool ok) { return __builtin_amdgcn_ballot_w64(!ok) == 0ull; }
DI v4u tfrag(const v4u* r, int i) {
  v4u o;
#pragma unroll
  for (int t = 0; t < 4; ++t) o[t] = __builtin_amdgcn_perm(r[2 * t + 1][i >> 1], r[2 * t][i >> 1], (i & 1) ? 0x07060302u : 0x05040100u);
  return o;
}
DI void transpose8(v4u* r) {
  v4u o[8];
#pragma unroll
  for (int i = 0; i < 8; ++i) o[i] = tfrag(r, i);
#pragma unroll
  for (int i = 0; i < 8; ++i) r[i] = o[i];
}

#define STAMP(i) do { SB(); stv[i] = (unsigned)__builtin_amdgcn_s_memrealtime(); SB(); } while (0)
#define STAMP_DECL unsigned stv[16]; _Pragma("unroll") for (int i_ = 0; i_ < 16; ++i_) stv[i_] = 0; const unsigned clk0_ = (unsigned)__builtin_amdgcn_s_memtime();
#define STAMP_FLUSH() do { { const unsigned c1_ = (unsigned)__builtin_amdgcn_s_memtime(), r1_ = (unsigned)__builtin_amdgcn_s_memrealtime(); stv[15] = stv[0] + (c1_ - clk0_) * 10u / (r1_ - stv[0]); } if (lane == 0) { _Pragma("unroll") for (int i_ = 0; i_ < 16; ++i_) ((unsigned*)((char*)p.ws + WS_ST))[(blockIdx.x * 8 + w) * 16 + i_] = stv[i_]; } } while (0)

template <int N, int SLEEP> DI void gather(v4u* v, rsrc_t r, int voff, int soff, unsigned tag) {
  unsigned a, t, u, cnt;
  if constexpr (N == 1) asm volatile(
      "s_mov_b32 %[cnt], %[cap]\n"
      "buffer_load_dwordx4 v[252:255], %[vo], %[rs], %[s0] offen offset:0 sc1\n"
      ".Lg_loop%=:\n"
      "s_waitcnt vmcnt(0)\n"
      "v_bitop3_b32 %[a], v253, v255, %[tag] bitop3:0x7e\n"
      "v_cmp_ne_u32 vcc, 0, %[a]\n"
      "s_cbranch_vccz .Lg_done%=\n"
      "s_sub_u32 %[cnt], %[cnt], 1\n"
      "s_cbranch_scc1 .Lg_done%=\n"
      "s_sleep %[sl]\n"
      "s_mov_b64 exec, vcc\n"
      "buffer_load_dwordx4 v[252:255], %[vo], %[rs], %[s0] offen offset:0 sc1\n"
      "s_mov_b64 exec, -1\n"
      "s_branch .Lg_loop%=\n"
      ".Lg_done%=:\n"
      : "=&{v[252:255]}"(v[0]), [a] "=&v"(a), [t] "=&v"(t), [u] "=&v"(u), [cnt] "=&s"(cnt)
      : [vo] "v"(voff), [rs] "s"(r), [tag] "s"(tag), [s0] "s"(soff + 0), [cap] "i"(SPIN_CAP), [sl] "i"(SLEEP) : "vcc", "scc", "memory");
  if constexpr (N == 2) asm volatile(
      "s_mov_b32 %[cnt], %[cap]\n"
      "buffer_load_dwordx4 v[248:251], %[vo], %[rs], %[s0] offen offset:0 sc1\n"
      "buffer_load_dwordx4 v[252:255], %[vo], %[rs], %[s0] offen offset:1024 sc1\n"
      ".Lg_loop%=:\n"
      "s_waitcnt vmcnt(0)\n"
      "v_bitop3_b32 %[a], v249, v251, %[tag] bitop3:0x7e\n"
      "v_bitop3_b32 %[t], v253, v255, %[tag] bitop3:0x7e\n"
      "v_or_b32 %[a], %[a], %[t]\n"
      "v_cmp_ne_u32 vcc, 0, %[a]\n"
      "s_cbranch_vccz .Lg_done%=\n"
      "s_sub_u32 %[cnt], %[cnt], 1\n"
      "s_cbranch_scc1 .Lg_done%=\n"
      "s_sleep %[sl]\n"
      "v_bitop3_b32 %[t], v249, v251, %[tag] bitop3:0x7e\n"
      "v_cmp_ne_u32 vcc, 0, %[t]\n"
      "s_mov_b64 exec, vcc\n"
      "buffer_load_dwordx4 v[248:251], %[vo], %[rs], %[s0] offen offset:0 sc1\n"
      "s_mov_b64 exec, -1\n"
      "v_bitop3_b32 %[t], v253, v255, %[tag] bitop3:0x7e\n"
      "v_cmp_ne_u32 vcc, 0, %[t]\n"
      "s_mov_b64 exec, vcc\n"
      "buffer_load_dwordx4 v[252:255], %[vo], %[rs], %[s0] offen offset:1024 sc1\n"
      "s_mov_b64 exec, -1\n"
      "s_branch .Lg_loop%=\n"
      ".Lg_done%=:\n"
      : "=&{v[248:251]}"(v[0]), "=&{v[252:255]}"(v[1]), [a] "=&v"(a), [t] "=&v"(t), [u] "=&v"(u), [cnt] "=&s"(cnt)
      : [vo] "v"(voff), [rs] "s"(r), [tag] "s"(tag), [s0] "s"(soff + 0), [cap] "i"(SPIN_CAP), [sl] "i"(SLEEP) : "vcc", "scc", "memory");
  if constexpr (N == 4) asm volatile(
      "s_mov_b32 %[cnt], %[cap]\n"
      "buffer_load_dwordx4 v[240:243], %[vo], %[rs], %[s0] offen offset:0 sc1\n"
      "buffer_load_dwordx4 v[244:247], %[vo], %[rs], %[s0] offen offset:1024 sc1\n"
      "buffer_load_dwordx4 v[248:251], %[vo], %[rs], %[s0] offen offset:2048 sc1\n"
      "buffer_load_dwordx4 v[252:255], %[vo], %[rs], %[s0] offen offset:3072 sc1\n"
      ".Lg_loop%=:\n"
      "s_waitcnt vmcnt(0)\n"
      "v_bitop3_b32 %[a], v241, v243, %[tag] bitop3:0x7e\n"
      "v_bitop3_b32 %[t], v245, v247, %[tag] bitop3:0x7e\n"
      "v_bitop3_b32 %[u], v249, v251, %[tag] bitop3:0x7e\n"
      "v_or3_b32 %[a], %[a], %[t], %[u]\n"
      "v_bitop3_b32 %[t], v253, v255, %[tag] bitop3:0x7e\n"
      "v_or_b32 %[a], %[a], %[t]\n"
      "v_cmp_ne_u32 vcc, 0, %[a]\n"
      "s_cbranch_vccz .Lg_done%=\n"
      "s_sub_u32 %[cnt], %[cnt], 1\n"
      "s_cbranch_scc1 .Lg_done%=\n"
      "s_sleep %[sl]\n"
      "v_bitop3_b32 %[t], v241, v243, %[tag] bitop3:0x7e\n"
      "v_cmp_ne_u32 vcc, 0, %[t]\n"
      "s_mov_b64 exec, vcc\n"
      "buffer_load_dwordx4 v[240:243], %[vo], %[rs], %[s0] offen offset:0 sc1\n"
      "s_mov_b64 exec, -1\n"
      "v_bitop3_b32 %[t], v245, v247, %[tag] bitop3:0x7e\n"
      "v_cmp_ne_u32 vcc, 0, %[t]\n"
      "s_mov_b64 exec, vcc\n"
      "buffer_load_dwordx4 v[244:247], %[vo], %[rs], %[s0] offen offset:1024 sc1\n"
      "s_mov_b64 exec, -1\n"
      "v_bitop3_b32 %[t], v249, v251, %[tag] bitop3:0x7e\n"
      "v_cmp_ne_u32 vcc, 0, %[t]\n"
      "s_mov_b64 exec, vcc\n"
      "buffer_load_dwordx4 v[248:251], %[vo], %[rs], %[s0] offen offset:2048 sc1\n"
      "s_mov_b64 exec, -1\n"
      "v_bitop3_b32 %[t], v253, v255, %[tag] bitop3:0x7e\n"
      "v_cmp_ne_u32 vcc, 0, %[t]\n"
      "s_mov_b64 exec, vcc\n"
      "buffer_load_dwordx4 v[252:255], %[vo], %[rs], %[s0] offen offset:3072 sc1\n"
      "s_mov_b64 exec, -1\n"
      "s_branch .Lg_loop%=\n"
      ".Lg_done%=:\n"
      : "=&{v[240:243]}"(v[0]), "=&{v[244:247]}"(v[1]), "=&{v[248:251]}"(v[2]), "=&{v[252:255]}"(v[3]), [a] "=&v"(a), [t] "=&v"(t), [u] "=&v"(u), [cnt] "=&s"(cnt)
      : [vo] "v"(voff), [rs] "s"(r), [tag] "s"(tag), [s0] "s"(soff + 0), [cap] "i"(SPIN_CAP), [sl] "i"(SLEEP) : "vcc", "scc", "memory");
  if constexpr (N == 8) asm volatile(
      "s_mov_b32 %[cnt], %[cap]\n"
      "buffer_load_dwordx4 v[224:227], %[vo], %[rs], %[s0] offen offset:0 sc1\n"
      "buffer_load_dwordx4 v[228:231], %[vo], %[rs], %[s0] offen offset:1024 sc1\n"
      "buffer_load_dwordx4 v[232:235], %[vo], %[rs], %[s0] offen offset:2048 sc1\n"
      "buffer_load_dwordx4 v[236:239], %[vo], %[rs], %[s0] offen offset:3072 sc1\n"
      "buffer_load_dwordx4 v[240:243], %[vo], %[rs], %[s1] offen offset:0 sc1\n"
      "buffer_load_dwordx4 v[244:247], %[vo], %[rs], %[s1] offen offset:1024 sc1\n"
      "buffer_load_dwordx4 v[248:251], %[vo], %[rs], %[s1] offen offset:2048 sc1\n"
      "buffer_load_dwordx4 v[252:255], %[vo], %[rs], %[s1] offen offset:3072 sc1\n"
      ".Lg_loop%=:\n"
      "s_waitcnt vmcnt(0)\n"
      "v_bitop3_b32 %[a], v225, v227, %[tag] bitop3:0x7e\n"
      "v_bitop3_b32 %[t], v229, v231, %[tag] bitop3:0x7e\n"
      "v_bitop3_b32 %[u], v233, v235, %[tag] bitop3:0x7e\n"
      "v_or3_b32 %[a], %[a], %[t], %[u]\n"
      "v_bitop3_b32 %[t], v237, v239, %[tag] bitop3:0x7e\n"
      "v_bitop3_b32 %[u], v241, v243, %[tag] bitop3:0x7e\n"
      "v_or3_b32 %[a], %[a], %[t], %[u]\n"
      "v_bitop3_b32 %[t], v245, v247, %[tag] bitop3:0x7e\n"
      "v_bitop3_b32 %[u], v249, v251, %[tag] bitop3:0x7e\n"
      "v_or3_b32 %[a], %[a], %[t], %[u]\n"
      "v_bitop3_b32 %[t], v253, v255, %[tag] bitop3:0x7e\n"
      "v_or_b32 %[a], %[a], %[t]\n"
      "v_cmp_ne_u32 vcc, 0, %[a]\n"
      "s_cbranch_vccz .Lg_done%=\n"
      "s_sub_u32 %[cnt], %[cnt], 1\n"
      "s_cbranch_scc1 .Lg_done%=\n"
      "s_sleep %[sl]\n"
      "v_bitop3_b32 %[t], v225, v227, %[tag] bitop3:0x7e\n"
      "v_cmp_ne_u32 vcc, 0, %[t]\n"
      "s_mov_b64 exec, vcc\n"
      "buffer_load_dwordx4 v[224:227], %[vo], %[rs], %[s0] offen offset:0 sc1\n"
      "s_mov_b64 exec, -1\n"
      "v_bitop3_b32 %[t], v229, v231, %[tag] bitop3:0x7e\n"
      "v_cmp_ne_u32 vcc, 0, %[t]\n"
      "s_mov_b64 exec, vcc\n"
      "buffer_load_dwordx4 v[228:231], %[vo], %[rs], %[s0] offen offset:1024 sc1\n"
      "s_mov_b64 exec, -1\n"
      "v_bitop3_b32 %[t], v233, v235, %[tag] bitop3:0x7e\n"
      "v_cmp_ne_u32 vcc, 0, %[t]\n"
      "s_mov_b64 exec, vcc\n"
      "buffer_load_dwordx4 v[232:235], %[vo], %[rs], %[s0] offen offset:2048 sc1\n"
      "s_mov_b64 exec, -1\n"
      "v_bitop3_b32 %[t], v237, v239, %[tag] bitop3:0x7e\n"
      "v_cmp_ne_u32 vcc, 0, %[t]\n"
      "s_mov_b64 exec, vcc\n"
      "buffer_load_dwordx4 v[236:239], %[vo], %[rs], %[s0] offen offset:3072 sc1\n"
      "s_mov_b64 exec, -1\n"
      "v_bitop3_b32 %[t], v241, v243, %[tag] bitop3:0x7e\n"
      "v_cmp_ne_u32 vcc, 0, %[t]\n"
      "s_mov_b64 exec, vcc\n"
      "buffer_load_dwordx4 v[240:243], %[vo], %[rs], %[s1] offen offset:0 sc1\n"
      "s_mov_b64 exec, -1\n"
      "v_bitop3_b32 %[t], v245, v247, %[tag] bitop3:0x7e\n"
      "v_cmp_ne_u32 vcc, 0, %[t]\n"
      "s_mov_b64 exec, vcc\n"
      "buffer_load_dwordx4 v[244:247], %[vo], %[rs], %[s1] offen offset:1024 sc1\n"
      "s_mov_b64 exec, -1\n"
      "v_bitop3_b32 %[t], v249, v251, %[tag] bitop3:0x7e\n"
      "v_cmp_ne_u32 vcc, 0, %[t]\n"
      "s_mov_b64 exec, vcc\n"
      "buffer_load_dwordx4 v[248:251], %[vo], %[rs], %[s1] offen offset:2048 sc1\n"
      "s_mov_b64 exec, -1\n"
      "v_bitop3_b32 %[t], v253, v255, %[tag] bitop3:0x7e\n"
      "v_cmp_ne_u32 vcc, 0, %[t]\n"
      "s_mov_b64 exec, vcc\n"
      "buffer_load_dwordx4 v[252:255], %[vo], %[rs], %[s1] offen offset:3072 sc1\n"
      "s_mov_b64 exec, -1\n"
      "s_branch .Lg_loop%=\n"
      ".Lg_done%=:\n"
      : "=&{v[224:227]}"(v[0]), "=&{v[228:231]}"(v[1]), "=&{v[232:235]}"(v[2]), "=&{v[236:239]}"(v[3]), "=&{v[240:243]}"(v[4]), "=&{v[244:247]}"(v[5]), "=&{v[248:251]}"(v[6]), "=&{v[252:255]}"(v[7]), [a] "=&v"(a), [t] "=&v"(t), [u] "=&v"(u), [cnt] "=&s"(cnt)
      : [vo] "v"(voff), [rs] "s"(r), [tag] "s"(tag), [s0] "s"(soff + 0), [s1] "s"(soff + 4096), [cap] "i"(SPIN_CAP), [sl] "i"(SLEEP) : "vcc", "scc", "memory");
  if constexpr (N == 12) asm volatile(
      "s_mov_b32 %[cnt], %[cap]\n"
      "buffer_load_dwordx4 v[208:211], %[vo], %[rs], %[s0] offen offset:0 sc1\n"
      "buffer_load_dwordx4 v[212:215], %[vo], %[rs], %[s0] offen offset:1024 sc1\n"
      "buffer_load_dwordx4 v[216:219], %[vo], %[rs], %[s0] offen offset:2048 sc1\n"
      "buffer_load_dwordx4 v[220:223], %[vo], %[rs], %[s0] offen offset:3072 sc1\n"
      "buffer_load_dwordx4 v[224:227], %[vo], %[rs], %[s1] offen offset:0 sc1\n"
      "buffer_load_dwordx4 v[228:231], %[vo], %[rs], %[s1] offen offset:1024 sc1\n"
      "buffer_load_dwordx4 v[232:235], %[vo], %[rs], %[s1] offen offset:2048 sc1\n"
      "buffer_load_dwordx4 v[236:239], %[vo], %[rs], %[s1] offen offset:3072 sc1\n"
      "buffer_load_dwordx4 v[240:243], %[vo], %[rs], %[s2] offen offset:0 sc1\n"
      "buffer_load_dwordx4 v[244:247], %[vo], %[rs], %[s2] offen offset:1024 sc1\n"
      "buffer_load_dwordx4 v[248:251], %[vo], %[rs], %[s2] offen offset:2048 sc1\n"
      "buffer_load_dwordx4 v[252:255], %[vo], %[rs], %[s2] offen offset:3072 sc1\n"
      ".Lg_loop%=:\n"
      "s_waitcnt vmcnt(0)\n"
      "v_bitop3_b32 %[a], v209, v211, %[tag] bitop3:0x7e\n"
      "v_bitop3_b32 %[t], v213, v215, %[tag] bitop3:0x7e\n"
      "v_bitop3_b32 %[u], v217, v219, %[tag] bitop3:0x7e\n"
      "v_or3_b32 %[a], %[a], %[t], %[u]\n"
      "v_bitop3_b32 %[t], v221, v223, %[tag] bitop3:0x7e\n"
      "v_bitop3_b32 %[u], v225, v227, %[tag] bitop3:0x7e\n"
      "v_or3_b32 %[a], %[a], %[t], %[u]\n"
      "v_bitop3_b32 %[t], v229, v231, %[tag] bitop3:0x7e\n"
      "v_bitop3_b32 %[u], v233, v235, %[tag] bitop3:0x7e\n"
      "v_or3_b32 %[a], %[a], %[t], %[u]\n"
      "v_bitop3_b32 %[t], v237, v239, %[tag] bitop3:0x7e\n"
      "v_bitop3_b32 %[u], v241, v243, %[tag] bitop3:0x7e\n"
      "v_or3_b32 %[a], %[a], %[t], %[u]\n"
      "v_bitop3_b32 %[t], v245, v247, %[tag] bitop3:0x7e\n"
      "v_bitop3_b32 %[u], v249, v251, %[tag] bitop3:0x7e\n"
      "v_or3_b32 %[a], %[a], %[t], %[u]\n"
      "v_bitop3_b32 %[t], v253, v255, %[tag] bitop3:0x7e\n"
      "v_or_b32 %[a], %[a], %[t]\n"
      "v_cmp_ne_u32 vcc, 0, %[a]\n"
      "s_cbranch_vccz .Lg_done%=\n"
      "s_sub_u32 %[cnt], %[cnt], 1\n"
      "s_cbranch_scc1 .Lg_done%=\n"
      "s_sleep %[sl]\n"
      "v_bitop3_b32 %[t], v209, v211, %[tag] bitop3:0x7e\n"
      "v_cmp_ne_u32 vcc, 0, %[t]\n"
      "s_mov_b64 exec, vcc\n"
      "buffer_load_dwordx4 v[208:211], %[vo], %[rs], %[s0] offen offset:0 sc1\n"
      "s_mov_b64 exec, -1\n"
      "v_bitop3_b32 %[t], v213, v215, %[tag] bitop3:0x7e\n"
      "v_cmp_ne_u32 vcc, 0, %[t]\n"
      "s_mov_b64 exec, vcc\n"
      "buffer_load_dwordx4 v[212:215], %[vo], %[rs], %[s0] offen offset:1024 sc1\n"
      "s_mov_b64 exec, -1\n"
      "v_bitop3_b32 %[t], v217, v219, %[tag] bitop3:0x7e\n"
      "v_cmp_ne_u32 vcc, 0, %[t]\n"
      "s_mov_b64 exec, vcc\n"
      "buffer_load_dwordx4 v[216:219], %[vo], %[rs], %[s0] offen offset:2048 sc1\n"
      "s_mov_b64 exec, -1\n"
      "v_bitop3_b32 %[t], v221, v223, %[tag] bitop3:0x7e\n"
      "v_cmp_ne_u32 vcc, 0, %[t]\n"
      "s_mov_b64 exec, vcc\n"
      "buffer_load_dwordx4 v[220:223], %[vo], %[rs], %[s0] offen offset:3072 sc1\n"
      "s_mov_b64 exec, -1\n"
      "v_bitop3_b32 %[t], v225, v227, %[tag] bitop3:0x7e\n"
      "v_cmp_ne_u32 vcc, 0, %[t]\n"
      "s_mov_b64 exec, vcc\n"
      "buffer_load_dwordx4 v[224:227], %[vo], %[rs], %[s1] offen offset:0 sc1\n"
      "s_mov_b64 exec, -1\n"
      "v_bitop3_b32 %[t], v229, v231, %[tag] bitop3:0x7e\n"
      "v_cmp_ne_u32 vcc, 0, %[t]\n"
      "s_mov_b64 exec, vcc\n"
      "buffer_load_dwordx4 v[228:231], %[vo], %[rs], %[s1] offen offset:1024 sc1\n"
      "s_mov_b64 exec, -1\n"
      "v_bitop3_b32 %[t], v233, v235, %[tag] bitop3:0x7e\n"
      "v_cmp_ne_u32 vcc, 0, %[t]\n"
      "s_mov_b64 exec, vcc\n"
      "buffer_load_dwordx4 v[232:235], %[vo], %[rs], %[s1] offen offset:2048 sc1\n"
      "s_mov_b64 exec, -1\n"
      "v_bitop3_b32 %[t], v237, v239, %[tag] bitop3:0x7e\n"
      "v_cmp_ne_u32 vcc, 0, %[t]\n"
      "s_mov_b64 exec, vcc\n"
      "buffer_load_dwordx4 v[236:239], %[vo], %[rs], %[s1] offen offset:3072 sc1\n"
      "s_mov_b64 exec, -1\n"
      "v_bitop3_b32 %[t], v241, v243, %[tag] bitop3:0x7e\n"
      "v_cmp_ne_u32 vcc, 0, %[t]\n"
      "s_mov_b64 exec, vcc\n"
      "buffer_load_dwordx4 v[240:243], %[vo], %[rs], %[s2] offen offset:0 sc1\n"
      "s_mov_b64 exec, -1\n"
      "v_bitop3_b32 %[t], v245, v247, %[tag] bitop3:0x7e\n"
      "v_cmp_ne_u32 vcc, 0, %[t]\n"
      "s_mov_b64 exec, vcc\n"
      "buffer_load_dwordx4 v[244:247], %[vo], %[rs], %[s2] offen offset:1024 sc1\n"
      "s_mov_b64 exec, -1\n"
      "v_bitop3_b32 %[t], v249, v251, %[tag] bitop3:0x7e\n"
      "v_cmp_ne_u32 vcc, 0, %[t]\n"
      "s_mov_b64 exec, vcc\n"
      "buffer_load_dwordx4 v[248:251], %[vo], %[rs], %[s2] offen offset:2048 sc1\n"
      "s_mov_b64 exec, -1\n"
      "v_bitop3_b32 %[t], v253, v255, %[tag] bitop3:0x7e\n"
      "v_cmp_ne_u32 vcc, 0, %[t]\n"
      "s_mov_b64 exec, vcc\n"
      "buffer_load_dwordx4 v[252:255], %[vo], %[rs], %[s2] offen offset:3072 sc1\n"
      "s_mov_b64 exec, -1\n"
      "s_branch .Lg_loop%=\n"
      ".Lg_done%=:\n"
      : "=&{v[208:211]}"(v[0]), "=&{v[212:215]}"(v[1]), "=&{v[216:219]}"(v[2]), "=&{v[220:223]}"(v[3]), "=&{v[224:227]}"(v[4]), "=&{v[228:231]}"(v[5]), "=&{v[232:235]}"(v[6]), "=&{v[236:239]}"(v[7]), "=&{v[240:243]}"(v[8]), "=&{v[244:247]}"(v[9]), "=&{v[248:251]}"(v[10]), "=&{v[252:255]}"(v[11]), [a] "=&v"(a), [t] "=&v"(t), [u] "=&v"(u), [cnt] "=&s"(cnt)
      : [vo] "v"(voff), [rs] "s"(r), [tag] "s"(tag), [s0] "s"(soff + 0), [s1] "s"(soff + 4096), [s2] "s"(soff + 8192), [cap] "i"(SPIN_CAP), [sl] "i"(SLEEP) : "vcc", "scc", "memory");
}
DI unsigned now() { return (unsigned)__builtin_amdgcn_s_memrealtime(); }
DI void pace(unsigned t0, unsigned dt) { while (now() - t0 < dt) __builtin_amdgcn_s_sleep(1); }
template <int KS> DI void out_gemv(const v4u* wf, lds_t ystage, rsrc_t ws, int dst, int slice_stride, unsigned tag, int lane) {
  const int c = lane & 15, g = lane >> 4;
  v4u a[KS];
#pragma unroll
  for (int t = 0; t < KS; ++t) a[t] = lds128(ystage + ((c >> 2) & 1) * (64 * KS) + 64 * t + 16 * g);
  f32x4 d[8];
#pragma unroll
  for (int i = 0; i < 8; ++i) d[i] = f32x4{0.f, 0.f, 0.f, 0.f};
#pragma unroll
  for (int t = 0; t < KS; ++t)
#pragma unroll
    for (int i = 0; i < 8; ++i) d[i] = mfma16(a[t], wf[8 * t + i], d[i]);
  const bool hi = lane >= 32;
  float v[4];
#pragma unroll
  for (int i = 0; i < 4; ++i) v[i] = hi ? d[4 + i][0] : d[i][0];
  const int voff = (lane >> 3) * slice_stride + (lane & 7) * 16;
  st_sc(v4u{fu(v[0]), tag, fu(v[1]), tag}, ws, voff, dst);
  st_sc(v4u{fu(v[2]), tag, fu(v[3]), tag}, ws, voff, dst + 8 * slice_stride);
}
template <int NL> DI void final_reduce(rsrc_t ws, int src, int slice, void* out, int colbase, unsigned tag, int lane) {
  v4u v[NL];
  if (NL == 8 && D_FA) __builtin_amdgcn_s_sleep(D_FA);
  gather<NL, NL == 8 ? SL_FA : 0>(v, ws, lane * 16, src, tag);
  float s0 = 0.f, s1 = 0.f;
#pragma unroll
  for (int i = 0; i < NL; ++i) { s0 += uf(v[i][0]); s1 += uf(v[i][2]); }
  s0 += dpp<ROR8>(s0); s1 += dpp<ROR8>(s1);
  s0 = x32_sum(x16_sum(s0)); s1 = x32_sum(x16_sum(s1));
  if (lane < 8) {
    const int l = 8 * (slice & 7) + lane, c = l & 15, g = l >> 4;
    const int col = 128 * (slice >> 4) + 8 * c + 4 * (g >> 1) + 2 * ((slice >> 3) & 1);
    *(unsigned*)((u16*)out + (g & 1) * 1024 + colbase + col) = pk(s0, s1);
  }
}

DI void role_a(const Params& p, int x, int i, int lane, int w) {
  STAMP_DECL
  STAMP(0);
  const unsigned t0 = now();
  const int b = x & 1, sp = (x >> 1) * 8 + i, h = 4 * ((i & 6) + (x & 1)) + (((x >> 1) + (i >> 1)) & 3), dh = i & 1;
  const int c = lane & 15, g = lane >> 4;
  const rsrc_t ws = mk(p.ws, WS_BYTES), rc = mk(p.ckv, 2 * 8192 * 1024);
  constexpr int IMG = 32768;
  const int kbase = (b * 8192 + sp * 256) * 1024;
  auto krow = [&](int row) { return kbase + ((row + 32 * i) & 255) * 1024; };
  if (w == 0) dma_sc(ws, 0, WS_EPOCH, 1024);
  {
    const int v0 = (lane ^ fsw(2 * w)) << 4, v1 = (lane ^ fsw(2 * w + 1)) << 4;
#pragma unroll
    for (int j = 0; j < 16; ++j) { const int row = 2 * w + (j & 1) + 16 * (j >> 1); dma_nt(rc, (j & 1) ? v1 : v0, krow(row), IMG + row * 1024); }
  }
  int ts = c * 1024 + 256 * g + fsw(c) * 16;
  const int hh = lane >> 5, blk = (lane >> 4) & 1, q4 = (lane & 15) >> 2, p4 = lane & 3;
  int ta[2][2];
#pragma unroll
  for (int ct = 0; ct < 2; ++ct)
#pragma unroll
    for (int t = 0; t < 2; ++t) { const int row = 8 * hh + 4 * t + q4; ta[ct][t] = IMG + row * 1024 + (((8 * w + 4 * ct + 2 * blk + (p4 >> 1)) ^ fsw(row)) << 4) + 8 * (p4 & 1); PIN(ta[ct][t]); }
  auto trfrag = [&](int ct, int ks) { const v2u r0 = ldstr(ta[ct][0] + ks * 16384), r1 = ldstr(ta[ct][1] + ks * 16384); return v4u{r0[0], r0[1], r1[0], r1[1]}; };
  int vv[4];
#pragma unroll
  for (int j = 0; j < 4; ++j) vv[j] = (lane ^ fsw(4 * (w & 3) + j)) << 4;
  v4u sa[16], pa[8][2];
  VMCNT(8);
  bar();
  STAMP(1);
  const unsigned tag = __builtin_amdgcn_readfirstlane(*LP(unsigned, 1024)) + 1u;
  if (w < 4) {
#pragma unroll
    for (int s = 0; s < 16; ++s) sa[s] = lds128(IMG + w * 16384 + (ts ^ (s << 4)));
#pragma unroll
    for (int s = 0; s < 16; ++s) PIN(sa[s]);
  }
#pragma unroll
  for (int ks = 0; ks < 4; ++ks) { pa[ks][0] = trfrag(0, ks); pa[ks][1] = trfrag(1, ks); }
#pragma unroll
  for (int ks = 0; ks < 4; ++ks) { PIN(pa[ks][0]); PIN(pa[ks][1]); }
  bar();
  if (w >= 4) {
#pragma unroll
    for (int j = 0; j < 16; ++j) { const int row = 4 * (w - 4) + (j & 3) + 16 * (j >> 2); dma_nt(rc, vv[j & 3], krow(128 + row), IMG + row * 1024); }
    VMCNT(16);
  } else {
    VMCNT(0);
  }
  bar();
  STAMP(2);
  PIN(ts);
  if (w >= 4) {
#pragma unroll
    for (int s = 0; s < 16; ++s) sa[s] = lds128(IMG + w * 16384 + (ts ^ (s << 4)));
#pragma unroll
    for (int s = 0; s < 16; ++s) PIN(sa[s]);
  }
#pragma unroll
  for (int ks = 4; ks < 8; ++ks) { pa[ks][0] = trfrag(0, ks); pa[ks][1] = trfrag(1, ks); }
#pragma unroll
  for (int ks = 4; ks < 8; ++ks) { PIN(pa[ks][0]); PIN(pa[ks][1]); }
  bar();
  STAMP(13);
  if (w >= 4) {
#pragma unroll
    for (int j = 16; j < 32; ++j) { const int row = 4 * (w - 4) + (j & 3) + 16 * (j >> 2); vmcnt<WBETA - 1>(); dma_nt(rc, vv[j & 3], krow(128 + row), IMG + row * 1024); }
    STAMP(3);
  } else if (w == 0) {
    for (int k = 0; k < QDELAY; ++k) __builtin_amdgcn_s_sleep(8);
    for (unsigned spins = 0;; ++spins) {
      const v4u f = ld_sc(ws, (lane >> 1) * FSTR + (lane & 1) * 16, WS_QFLAG);
      if (all_ok(f[0] == tag && f[1] == ~tag && f[2] == tag && f[3] == ~tag) || spins >= SPIN_CAP) break;
      asm volatile("" ::: "memory");
      __builtin_amdgcn_s_sleep(2);
    }
    STAMP(3);
#pragma unroll
    for (int j = 0; j < 32; ++j) dma_sc(ws, (lane ^ fsw(j)) << 4, WS_QABS + b * 32768 + j * 1024, j * 1024);
  }
  VMCNT(0);
  bar();
  STAMP(4);
  PIN(ts);
  f32x4 d[2][2];
#pragma unroll
  for (int k = 0; k < 4; ++k) d[k >> 1][k & 1] = f32x4{0.f, 0.f, 0.f, 0.f};
  v4u wv[8], wo[16];
  const rsrc_t rv = mk(p.wuv, 512 * 8192);
  const int vo = (lane >> 3) * 8 * 8192 + (lane & 7) * 16;
  const rsrc_t ro = mk(p.woa, 4096 * 1024);
  auto ld_wo = [&](int n) { return ld_nt(ro, g * 8192 + c * 16, (h * 128 + dh * 64 + 32 * (n >> 3) + (n & 7)) * 1024 + w * 256); };
  {
    v4u q0[3], q1[3], sb[3];
#pragma unroll
    for (int s = 0; s < 18; ++s) {
      if (s < 16) { const int a = ts ^ (s << 4); q0[s % 3] = lds128(a); q1[s % 3] = lds128(a + 16384); sb[s % 3] = lds128(a + (IMG + w * 16384)); }
      SB();
      if (s >= 2) {
        const int t = s - 2;
        d[0][0] = mfma16(sa[t], q0[t % 3], d[0][0]); d[0][1] = mfma16(sa[t], q1[t % 3], d[0][1]);
        d[1][0] = mfma16(sb[t % 3], q0[t % 3], d[1][0]); d[1][1] = mfma16(sb[t % 3], q1[t % 3], d[1][1]);
        { const int k = t < 3 ? t : (t % 3 == 2 ? 2 + t / 3 : -1);
          if (k >= 0) wv[k] = ld_nt(rv, vo, (64 * w + k) * 8192 + h * 256 + dh * 128); }
      }
      SB();
    }
  }
  float m[2];
#pragma unroll
  for (int hg = 0; hg < 2; ++hg) {
    float t = fmaxf(fmaxf(fmaxf(d[0][hg][0], d[0][hg][1]), fmaxf(d[0][hg][2], d[0][hg][3])), fmaxf(fmaxf(d[1][hg][0], d[1][hg][1]), fmaxf(d[1][hg][2], d[1][hg][3])));
    m[hg] = x32_max(x16_max(t));
  }
  constexpr int PST = 528, MX = 17408, LS = 18432, WT = 19456;
  bar();
  STAMP(5);
  if (g < 2) *LP(float, MX + ((16 * g + c) * 8 + w) * 4) = g ? m[1] : m[0];
  bar();
  if ((PRE & 4) && w == 6 && !(i & 1)) dma_sc(ws, lane * 16, WS_AML + (b * 32 + sp) * 512, 24576);
  if (PRE & 8) dma_sc(ws, lane * 16, WS_OPA + (x * 8 + i) * 8192 + w * 1024, 24576);
  wv[7] = ld_nt(rv, vo, (64 * w + 7) * 8192 + h * 256 + dh * 128);
#pragma unroll
  for (int hg = 0; hg < 2; ++hg) {
    const f32x4 a0 = *LP(f32x4, MX + (16 * hg + c) * 32), a1 = *LP(f32x4, MX + (16 * hg + c) * 32 + 16);
    m[hg] = fmaxf(fmaxf(fmaxf(a0[0], a0[1]), fmaxf(a0[2], a0[3])), fmaxf(fmaxf(a1[0], a1[1]), fmaxf(a1[2], a1[3])));
  }
  float l[2];
#pragma unroll
  for (int hg = 0; hg < 2; ++hg) {
    const float mc = -m[hg] * C2;
    float s = 0.f;
#pragma unroll
    for (int ab = 0; ab < 2; ++ab) {
      float e[4];
#pragma unroll
      for (int r = 0; r < 4; ++r) { e[r] = ex2(fmaf(d[ab][hg][r], C2, mc)); s += e[r]; }
      *LP(v2u, (16 * hg + c) * PST + (128 * ab + 16 * w + 4 * g) * 2) = v2u{pk(e[0], e[1]), pk(e[2], e[3])};
    }
    l[hg] = x32_sum(x16_sum(s));
  }
  if (g < 2) *LP(float, LS + ((16 * g + c) * 8 + w) * 4) = g ? l[1] : l[0];
  const float Mh = (g & 1) ? m[1] : m[0];
  bar();
  STAMP(6);
  f32x16 o[2];
#pragma unroll
  for (int k = 0; k < 16; ++k) { o[0][k] = 0.f; o[1][k] = 0.f; }
  {
    int pb = (lane & 31) * PST + 16 * hh;
    PIN(pb); PIN(ta[0][0]); PIN(ta[0][1]); PIN(ta[1][0]); PIN(ta[1][1]);
    v4u pf[3], tb[3][2];
#pragma unroll
    for (int s = 0; s < 18; ++s) {
      if (s < 16) { pf[s % 3] = lds128(pb + 32 * s); if (s >= 8) { tb[s % 3][0] = trfrag(0, s - 8); tb[s % 3][1] = trfrag(1, s - 8); } }
      SB();
      if (s >= 2) {
        const int t = s - 2;
        o[0] = mfma32(t < 8 ? pa[t][0] : tb[t % 3][0], pf[t % 3], o[0]);
        o[1] = mfma32(t < 8 ? pa[t][1] : tb[t % 3][1], pf[t % 3], o[1]);
        if (w < 4 && !(t & 1) && (t >> 1) < WO_EARLY) wo[t >> 1] = ld_wo(t >> 1);
      }
      SB();
    }
  }
  float Lh;
  { const f32x4 a0 = *LP(f32x4, LS + (lane & 31) * 32), a1 = *LP(f32x4, LS + (lane & 31) * 32 + 16); Lh = ((a0[0] + a0[1]) + (a0[2] + a0[3])) + ((a1[0] + a1[1]) + (a1[2] + a1[3])); }
  bar();
  STAMP(7);
  {
    const lds_t stg = IMG + w * 4608;
#pragma unroll
    for (int ct = 0; ct < 2; ++ct)
#pragma unroll
      for (int rq = 0; rq < 4; ++rq)
        *LP(v2u, stg + (lane & 31) * 144 + (32 * ct + 8 * rq + 4 * hh) * 2) = v2u{pk(o[ct][4 * rq], o[ct][4 * rq + 1]), pk(o[ct][4 * rq + 2], o[ct][4 * rq + 3])};
    asm volatile("" ::: "memory");
    v4u v[4];
#pragma unroll
    for (int e = 0; e < 4; ++e) v[e] = lds128(stg + (8 * e + (lane >> 3)) * 144 + (lane & 7) * 16);
    SB();
#pragma unroll
    for (int e = 0; e < 4; ++e) {
      st_sc(v[e], ws, (lane >> 3) * 65536 + (lane & 7) * 16, WS_AO + e * 8 * 65536 + (b * 32 + sp) * 1024 + w * 128);
    }
  }
  VMCNT(0);
  bar();
  STAMP(8);
  if (w == 7 && lane < 32) st_sc(v4u{fu(Mh), tag, fu(Lh), tag}, ws, lane * 16, WS_AML + (b * 32 + sp) * 512);
  if (!TWA && w < 4) {
#pragma unroll
    for (int n = WO_EARLY; n < 16; ++n) wo[n] = ld_wo(n);
  }
  SB();
  transpose8(wv);
#pragma unroll
  for (int n = 0; n < 8; ++n) PIN(wv[n]);
  constexpr int XS = IMG + 40960, G1 = XS + 8448, YS = G1 + 8192;
  {
    v4u f;
    if (D_AML) __builtin_amdgcn_s_sleep(D_AML);
    gather<1, SL_AML>(&f, ws, (lane & 7) * 512, WS_AML + w * 4096 + h * 16, tag);
    STAMP(9);
    v4u t[8];
#pragma unroll
    for (int k = 0; k < 8; ++k) t[k] = ld_sc(ws, lane * 16 + (k & 3) * 1024, WS_AO + h * 65536 + w * 8192 + (k >> 2) * 4096);
    SB();
    const float mm = uf(f[0]);
    float M = fmaxf(mm, dpp<XOR1>(mm)); M = fmaxf(M, dpp<XOR2>(M)); M = fmaxf(M, dpp<ROR4>(M));
    const float e = ex2((mm - M) * C2);
    float Lw = e * uf(f[2]); Lw += dpp<XOR1>(Lw); Lw += dpp<XOR2>(Lw); Lw += dpp<ROR4>(Lw);
    if (lane == 0) *LP(f32x2, WT + w * 8) = f32x2{M, Lw};
    if (!TWA && w < 4) { transpose8(wo); transpose8(wo + 8); }
    float xs[8];
#pragma unroll
    for (int j = 0; j < 8; ++j) xs[j] = 0.f;
#pragma unroll
    for (int k = 0; k < 8; ++k) {
      const float wt = uf(__builtin_amdgcn_readlane(fu(e), k));
#pragma unroll
      for (int j = 0; j < 4; ++j) { xs[2 * j] = fmaf(wt, bflo(t[k][j]), xs[2 * j]); xs[2 * j + 1] = fmaf(wt, bfhi(t[k][j]), xs[2 * j + 1]); }
    }
    *LP(v4u, XS + w * 1056 + lane * 16) = v4u{pk(xs[0], xs[1]), pk(xs[2], xs[3]), pk(xs[4], xs[5]), pk(xs[6], xs[7])};
  }
  bar();
  STAMP(10);
  if (TWA && w < 4) {
    pace(t0, TWA);
#pragma unroll
    for (int n = WO_EARLY; n < 16; ++n) wo[n] = ld_wo(n);
    SB();
  }
  {
    const v4u xa = lds128(XS + (4 * (c >> 3) + (c & 3)) * 1056 + (8 * w + 2 * g + ((c >> 2) & 1)) * 16);
    const f32x4 m0 = *LP(f32x4, WT + hh * 32), m1 = *LP(f32x4, WT + hh * 32 + 16);
    const float M = fmaxf(fmaxf(m0[0], m0[2]), fmaxf(m1[0], m1[2]));
    f32x4 fs = {ex2((m0[0] - M) * C2), ex2((m0[2] - M) * C2), ex2((m1[0] - M) * C2), ex2((m1[2] - M) * C2)};
    fs *= 1.0f / (fs[0] * m0[1] + fs[1] * m0[3] + fs[2] * m1[1] + fs[3] * m1[3]);
    float y[8];
#pragma unroll
    for (int jj = 0; jj < 8; ++jj) { const f32x4 dd = mfma16(xa, wv[jj], f32x4{0.f, 0.f, 0.f, 0.f}); y[jj] = (fs[0] * dd[0] + fs[1] * dd[1]) + (fs[2] * dd[2] + fs[3] * dd[3]); }
    if (((lane >> 3) & 1) == (g & 1)) {
      const lds_t dst = G1 + ((2 * w + (g & 1)) * 128 + (lane >> 5) * 64 + 8 * (lane & 7)) * 4;
      *LP(f32x4, dst) = f32x4{y[0], y[1], y[2], y[3]};
      *LP(f32x4, dst + 16) = f32x4{y[4], y[5], y[6], y[7]};
    }
  }
  bar();
  STAMP(11);
  if (w < 4) {
    f32x2 s = {0.f, 0.f};
#pragma unroll
    for (int k = 0; k < 16; ++k) s += *LP(f32x2, G1 + k * 512 + lane * 8);
    const lds_t ys = YS + w * 256;
    *LP(unsigned, ys + lane * 4) = pk(s[0], s[1]);
    asm volatile("" ::: "memory");
    if (TWA) { transpose8(wo); transpose8(wo + 8); }
    out_gemv<2>(wo, ys, ws, WS_OPA + w * 16 * 8192 + (2 * h + dh) * 128, 8192, tag, lane);
  } else if (w == 4) {
    const int slice = x * 8 + i;
    final_reduce<8>(ws, WS_OPA + slice * 8192, slice, p.out, 0, tag, lane);
  } else if (w == 5 && x == 0 && i == 0) {
    for (unsigned spins = 0;; ++spins) {
      const v4u f = ld_sc(ws, lane * 256, WS_QB);
      if (all_ok(tags_ok(f, tag)) || spins >= SPIN_CAP) break;
      asm volatile("" ::: "memory");
      __builtin_amdgcn_s_sleep(1);
    }
    if (lane == 0) __builtin_amdgcn_raw_buffer_store_b32(tag, ws, 0, WS_EPOCH, AUX_SC1);
  }
  STAMP(12);
  STAMP_FLUSH();
}

constexpr int B_X = 65536, B_QB = 67584, B_QA = 68608, B_ML = 69120, B_E = 69376, B_CNT = 69632, B_WT = 69888, B_EP = 70656, B_KS = 73728;
#define ST_ARG , unsigned* stv
#define ST_PASS , stv

DI void k_write(const v4u& kr, int j, lds_t my, int c, int g) { const int key = 4 * j + g; *LP(v4u, my + key * 256 + ((c ^ (key & 15)) << 4)) = kr; }
DI void k_read(v4u* kr, lds_t my, int c, int g) {
  const int k0 = 4 * (c & 3) + (c >> 2);
#pragma unroll
  for (int T = 0; T < 2; ++T)
#pragma unroll
    for (int s = 0; s < 4; ++s) kr[4 * T + s] = lds128(my + (16 * T + k0) * 256 + (((4 * s + g) ^ k0) << 4));
}
DI unsigned lds_arrive(lds_t cnt, int lane) {
  unsigned old = 0;
  asm volatile("s_waitcnt lgkmcnt(0)" ::: "memory");
  if (lane == 0) old = __hip_atomic_fetch_add(LP(unsigned, cnt), 1u, __ATOMIC_RELAXED, __HIP_MEMORY_SCOPE_WORKGROUP);
  old = __builtin_amdgcn_readfirstlane(old);
  asm volatile("" ::: "memory");
  return old;
}
template <int NU> DI void b_scores(const v4u* kr, v4u* pf, rsrc_t ws, int w, int lane, int nwaves, int mlbase, unsigned tag) {
  const int c = lane & 15, g = lane >> 4;
  v4u qf[4];
#pragma unroll
  for (int s = 0; s < 4; ++s) qf[s] = lds128(B_QB + (c >> 2) * 256 + 64 * s + 16 * g);
  float sc[8 * NU];
#pragma unroll
  for (int T = 0; T < 2 * NU; ++T) {
    f32x4 d = {0.f, 0.f, 0.f, 0.f};
#pragma unroll
    for (int s = 0; s < 4; ++s) d = mfma16(kr[4 * T + s], qf[s], d);
#pragma unroll
    for (int r = 0; r < 4; ++r) sc[4 * T + r] = d[r];
  }
  float m = sc[0];
#pragma unroll
  for (int k = 1; k < 8 * NU; ++k) m = fmaxf(m, sc[k]);
  m = x32_max(x16_max(m));
  float l = 0.f;
  const float mc = -m * C2;
#pragma unroll
  for (int k = 0; k < 8 * NU; ++k) { sc[k] = ex2(fmaf(sc[k], C2, mc)); l += sc[k]; }
  l = x32_sum(x16_sum(l));
#pragma unroll
  for (int u = 0; u < NU; ++u)
#pragma unroll
    for (int t = 0; t < 4; ++t) pf[u][t] = pk(sc[8 * u + 2 * t], sc[8 * u + 2 * t + 1]);
  if (lane < 16 && (c & 3) == 0) *LP(f32x2, B_ML + (w * 4 + (c >> 2)) * 8) = f32x2{m, l};
  if ((int)lds_arrive(B_CNT, lane) == nwaves - 1) {
    const f32x2 ml = *LP(f32x2, B_ML + (lane & 31) * 8);
    float M = ml[0];
    M = fmaxf(M, dpp<ROR4>(M)); M = fmaxf(M, dpp<ROR8>(M)); M = x16_max(M);
    const float e = ex2((ml[0] - M) * C2);
    float t = ml[1] * e;
    t += dpp<ROR4>(t); t += dpp<ROR8>(t); t = x16_sum(t);
    if (lane < 32) *LP(float, B_E + ((lane & 3) * 8 + (lane >> 2)) * 4) = e;
    if (lane < 8) st_sc(v4u{fu(M), tag, fu(t), tag}, ws, lane * 16, mlbase);
  }
}
DI void b_pv(const v4u& pf, const v4u* vr, f32x4* acc) {
  v4u t[8];
#pragma unroll
  for (int j = 0; j < 4; ++j) {
#pragma unroll
    for (int k = 0; k < 8; ++k) t[k][j] = __builtin_amdgcn_perm(vr[2 * j + 1][k >> 1], vr[2 * j][k >> 1], (k & 1) ? 0x07060302u : 0x05040100u);
    SB();
  }
#pragma unroll
  for (int k = 0; k < 8; ++k) acc[k] = mfma16(pf, t[k], acc[k]);
#pragma unroll
  for (int k = 0; k < 8; ++k) PIN(acc[k]);
}
DI void b_merge(const f32x4* acc, rsrc_t ws, int w, int lane, int nwaves, int obase, unsigned tag ST_ARG) {
  const lds_t mo = B_KS + w * 8192 + lane * 32;
  *LP(f32x4, mo) = f32x4{acc[0][0], acc[1][0], acc[2][0], acc[3][0]};
  *LP(f32x4, mo + 16) = f32x4{acc[4][0], acc[5][0], acc[6][0], acc[7][0]};
  if ((int)lds_arrive(B_CNT + 4, lane) == nwaves - 1) {
    int ob = B_KS + lane * 16, eb = B_E + (lane >> 5) * 32;
    PIN(ob); PIN(eb);
    f32x4 e[2][2], v[2][8];
#pragma unroll
    for (int eh = 0; eh < 2; ++eh) {
      e[eh][0] = *LP(f32x4, eb + eh * 64); e[eh][1] = *LP(f32x4, eb + eh * 64 + 16);
#pragma unroll
      for (int k = 0; k < 8; ++k) v[eh][k] = *LP(f32x4, ob + k * 8192 + eh * 1024);
    }
    SB();
#pragma unroll
    for (int eh = 0; eh < 2; ++eh) {
      f32x4 s = e[eh][0][0] * v[eh][0];
#pragma unroll
      for (int k = 1; k < 8; ++k) s += e[eh][k >> 2][k & 3] * v[eh][k];
      st_sc(v4u{pk(s[0], s[1]), tag, pk(s[2], s[3]), tag}, ws, (lane >> 5) * 12288 + (lane & 31) * 16, obase + eh * 2 * 12288);
    }
    STAMP(8);
  }
}
DI void lds_signal(lds_t cnt, int lane) { if (lane == 0) (void)__hip_atomic_fetch_add(LP(unsigned, cnt), 1u, __ATOMIC_RELAXED, __HIP_MEMORY_SCOPE_WORKGROUP); }
DI void lds_wait(lds_t cnt, unsigned want) {
  for (unsigned spins = 0; *(volatile LAS unsigned*)(size_t)cnt < want && spins < LSPIN; ++spins) __builtin_amdgcn_s_sleep(1);
  asm volatile("" ::: "memory");
}
template <int NCT> DI void gemv_n(f32x4* d, int ct0, lds_t wb, lds_t xb, int c, int g) {
  const int q4 = c >> 2, p4 = c & 3, fx = (q4 >> 1) | ((g & 1) << 1);
  int a[NCT];
#pragma unroll
  for (int ct = 0; ct < NCT; ++ct) { a[ct] = wb + (8 * g + q4) * 128 + (((ct0 + ct) ^ fx) << 5) + 8 * p4; PIN(a[ct]); }
  int xa = xb + (c & 1) * 512 + 16 * g;
  PIN(xa);
  constexpr int D = NCT == 1 ? 4 : 1;
  v4u xf[D + 1], wf[D + 1][NCT];
#pragma unroll
  for (int s = 0; s < 8 + D; ++s) {
    if (s < 8) {
      xf[s % (D + 1)] = lds128(xa + 64 * s);
#pragma unroll
      for (int ct = 0; ct < NCT; ++ct) { const v2u r0 = ldstr(a[ct] + 4096 * s), r1 = ldstr(a[ct] + 4096 * s + 512); wf[s % (D + 1)][ct] = v4u{r0[0], r0[1], r1[0], r1[1]}; }
    }
    SB();
    if (s >= D) {
#pragma unroll
      for (int ct = 0; ct < NCT; ++ct) d[ct] = mfma16(wf[(s - D) % (D + 1)][ct], xf[(s - D) % (D + 1)], d[ct]);
    }
    SB();
  }
}

template <int NU, int LS, int WIN>
DI void b_stream(unsigned t0, rsrc_t rk, rsrc_t rv, rsrc_t ws, int ub, int w, int lane, int nwaves, int obase, int mlbase ST_ARG) {
  const int c = lane & 15, g = lane >> 4;
  const lds_t my = B_KS + w * 8192;
  constexpr int NL = 8 * NU;
  auto off = [&](int n) { return NU == 1 ? ub + n * LS : ub + (n / GI) * (12 * GI * LS) + (n % GI) * LS; };
  v4u kr[NL], vr[NL], pf[NU];
  f32x4 acc[8];
#pragma unroll
  for (int k = 0; k < 8; ++k) acc[k] = f32x4{0.f, 0.f, 0.f, 0.f};
#pragma unroll
  for (int n = 0; n < NL + WQK; ++n) {
    if (n < WIN) vmcnt<WIN - 1>();
    if (PB && NU > 1 && n >= 8 && n % PSTEP == 0) pace(t0, PA + n * PB / 8);
    if (n < NL) kr[n] = ld_nt(rk, lane * 16, off(n));
    else vr[n - NL] = ld_nt(rv, lane * 16, off(n - NL));
    SB();
    const int t = n + 1 - WIN;
    if (t >= 0 && t < NL) { k_write(kr[t], t & 7, my, c, g); if ((t & 7) == 7) k_read(kr + (t - 7), my, c, g); }
    SB();
  }
  STAMP(3);
  lds_wait(B_CNT + 8, 1u);
  const unsigned tag = __builtin_amdgcn_readfirstlane(*LP(unsigned, B_EP)) + 1u;
  STAMP(4);
  b_scores<NU>(kr, pf, ws, w, lane, nwaves, mlbase, tag);
  SB();
  STAMP(5);
#pragma unroll
  for (int n = WQK; n < NL; ++n) {
    vmcnt<WIN - 1>();
    if (PB && n % PSTEP == 0) pace(t0, PA + (NL + n) * PB / 8);
    vr[n] = ld_nt(rv, lane * 16, off(n));
    SB();
    if (n == 13) { b_pv(pf[0], vr, acc); SB(); }
  }
  STAMP(6);
  if (NU > 1) lds_signal(B_CNT + 24, lane);
  if (NU > 1) { b_pv(pf[NU - 2], vr + 8 * (NU - 2), acc); SB(); }
  b_pv(pf[NU - 1], vr + 8 * (NU - 1), acc);
  SB();
  STAMP(7);
  b_merge(acc, ws, w, lane, nwaves, obase, tag ST_PASS);
}
DI void fetch_qb(unsigned t0, rsrc_t ws, int src, unsigned tag, int lane) {
  if (QBT) pace(t0, QBT);
  v4u f[2];
  gather<2, 4>(f, ws, lane * 16, src, tag);
  const v4u f0 = f[0], f1 = f[1];
  *LP(v2u, B_QB + lane * 8) = v2u{f0[0], f0[2]};
  *LP(v2u, B_QB + 512 + lane * 8) = v2u{f1[0], f1[2]};
  asm volatile("s_waitcnt lgkmcnt(0)" ::: "memory");
  if (lane == 0) *(volatile LAS unsigned*)(size_t)(B_CNT + 8) = 1u;
}

DI void role_b(const Params& p, int x, int j, int lane, int w) {
  STAMP_DECL
  STAMP(0);
  const unsigned t0 = now();
  const int type = j % 3, k3 = j / 3, b = j / 12, sp = j % 12;
  const bool tq = type != 2;
  const int c = lane & 15, g = lane >> 4;
  const int col = 8 * k3 + ((x + k3) & 7);
  const rsrc_t ws = mk(p.ws, WS_BYTES);
  const rsrc_t rw = mk(tq ? p.wqa : p.wqb, 512 * 8192);
  const int sb = (type & 1) * 256 * 8192 + col * 128;
  const int vn0 = (lane >> 3) * 8192 + (((lane & 7) ^ ((g & 1) << 1)) << 4), vn1 = vn0 ^ 64;
  auto dma_w = [&](int n) {
    dma_nt(rw, (n & 1) ? vn1 : vn0, sb + ((n >> 2) * 256 + 32 * w + 8 * (n & 3)) * 8192, (n >> 2) * 32768 + (4 * w + (n & 3)) * 1024);
  };
  const int hq = col >> 1, hu = (hq & ~3) | ((hq + 2) & 3), cq = 2 * (col & 1) + type;
  constexpr int DUMMY = 71680;
  if (tq) {
#pragma unroll
    for (int n = 0; n < 4; ++n) dma_w(n);
  } else {
    const int ka = (3 - x - ((unsigned)(size_t)p.wqa >> 7)) & 7, kb = (3 - x - ((unsigned)(size_t)p.wqb >> 7)) & 7;
    const int vp = (lane >> 3) * 8192 + (lane & 7) * 16, sp0 = (8 * k3 + w) * 8 * 8192;
    dma_df(mk(p.wqa, 512 * 8192), vp, sp0 + (8 * ka + ((x + ka) & 7)) * 128, DUMMY);
    dma_df(rw, vp, sp0 + (8 * kb + ((x + kb) & 7)) * 128, DUMMY);
    dma_w(0); dma_w(1);
  }
  if (w == 0) {
    const rsrc_t rx = mk(tq ? p.xa : p.xb, 2048);
    const int vx = (lane >> 5) * 1024 + (lane & 31) * 16;
    dma_df(rx, vx, (type & 1) * 512, B_X); dma_df(rx, vx, 512, B_X + 1024);
    dma_sc(ws, 0, WS_EPOCH, B_EP);
    if ((PRE & 1) && tq) dma_sc(ws, lane * 16, WS_QA + hu * 4096 + cq * 1024, DUMMY);
    if ((PRE & 1) && !tq && k3 == 0) dma_sc(ws, lane * 16, WS_QFLAG, DUMMY);
    if (lane < 8) *LP(unsigned, B_CNT + lane * 4) = 0u;
    if (lane < 4) *LP(f32x2, B_ML + lane * 8) = f32x2{-INFINITY, 0.f};
    *LP(f32x4, B_KS + lane * 32) = f32x4{0.f, 0.f, 0.f, 0.f}; *LP(f32x4, B_KS + lane * 32 + 16) = f32x4{0.f, 0.f, 0.f, 0.f};
  }
  const rsrc_t rk = mk(p.kc, 16 * 8192 * 256), rv = mk(p.vc, 16 * 8192 * 256);
  const int pbase = (b * 8 + x) * 8192 * 256;
  const int obase = WS_BO + 4 * x * 12288 + sp * 1024 + b * 512, mlbase = WS_BML + x * 4096 + b * 2048 + sp * 128;
  if (!tq) {
    bar();
#pragma unroll
    for (int n = 2; n < 8; ++n) { VMCNT(4); dma_w(n); }
    VMCNT(4);
    if (w > 0) {
      lds_signal(B_CNT + 12, lane);
      STAMP(1);
      const rsrc_t rc = mk(p.ckv, 2 * 8192 * 1024);
      const int cb = ((x & 1) * 8192 + (x >> 1) * 2048 + k3 * 256) * 1024;
#pragma unroll
      for (int s = 0; s < PFN; ++s) {
        VMCNT(4);
        if (s == 4) { lds_signal(B_CNT + 16, lane); STAMP(2); }
        dma_df(rc, lane * 16, cb + ((256 - 7 * PFN + 7 * s + w - 1 + 32 * k3) & 255) * 1024, DUMMY);
      }
    }
    STAMP(12);
    {
      int cs = 0;
#pragma unroll
      for (int i = 0; i < 8; ++i) { const int cc = 2 * (4 * ((i & 6) + (x & 1)) + (((x >> 1) + (i >> 1)) & 3)) + (i & 1); if (((cc + ((unsigned)(size_t)p.wuv >> 7)) & 7) == 3) cs = cc; }
      if (w > 0) dma_df(mk(p.wuv, 512 * 8192), (lane >> 3) * 8192 + (lane & 7) * 16, (8 * k3 + w) * 8 * 8192 + cs * 128, DUMMY);
    }
  } else {
    bar();
    const rsrc_t ru = mk(p.wuk, 512 * 8192);
    const int vt = g * 8192 + ((c ^ (g | ((w & 1) << 3))) << 4);
#pragma unroll
    for (int n = 0; n < 4; ++n) { VMCNT(3); dma_nt(ru, vt ^ ((n >> 1) << 6), (cq * 128 + 16 * w + 4 * n) * 8192 + hu * 256, 32768 + (4 * w + n) * 1024); }
    VMCNT(4);
    bar();
    STAMP(1);
    if (w >= 4) {
      const unsigned tag = __builtin_amdgcn_readfirstlane(*LP(unsigned, B_EP)) + 1u;
      f32x4 d = {0.f, 0.f, 0.f, 0.f};
      gemv_n<1>(&d, w - 4, 0, B_X, c, g);
      if (c < 4) st_sc(v4u{fu(c < 2 ? d[0] : d[2]), tag, fu(c < 2 ? d[1] : d[3]), tag}, ws, (c & 1) * 1024 + g * 32 + (c >> 1) * 16, WS_QA + hq * 4096 + type * 2048 + (col & 1) * 512 + (w - 4) * 128);
    }
    VMCNT(0);
    bar();
    STAMP(12);
    if (w > 0) { lds_wait(B_CNT + 20, 1u); STAMP(9); }
  }
  if (w > 0) {
    b_stream<3, 7168, W1>(t0, rk, rv, ws, pbase + sp * GI * 7168 + (w - 1) * 1024, w, lane, tq ? 7 : 8, obase, mlbase ST_PASS);
    STAMP_FLUSH();
    return;
  }
  if (!tq) {
    lds_wait(B_CNT + 12, 7u);
    STAMP(1);
    const unsigned tag = __builtin_amdgcn_readfirstlane(*LP(unsigned, B_EP)) + 1u;
    f32x4 d[4];
#pragma unroll
    for (int ct = 0; ct < 4; ++ct) d[ct] = f32x4{0.f, 0.f, 0.f, 0.f};
#pragma unroll 1
    for (int part = 0; part < 2; ++part) {
      if (part) { VMCNT(0); lds_wait(B_CNT + 16, 7u); STAMP(2); }
      gemv_n<4>(d, 0, part * 32768, B_X + part * 1024, c, g);
    }
    const int ct = (c >> 1) & 3;
    const f32x4 dd = ct == 0 ? d[0] : ct == 1 ? d[1] : ct == 2 ? d[2] : d[3];
    if (c < 8) st_sc(v4u{pk(dd[0], dd[1]), tag, pk(dd[2], dd[3]), tag}, ws, (c & 1) * 16384 + ct * 64 + g * 16, WS_QB + col * 256);
    STAMP(11);
    fetch_qb(t0, ws, WS_QB + (b * 8 + x) * 2048, tag, lane);
    STAMP(13);
    if (PRE & 16) {
#pragma unroll
      for (int n = 0; n < 4; ++n) dma_sc(ws, lane * 16, WS_OPB + (8 * x + k3) * 4096 + n * 1024, DUMMY);
    }
    b_stream<1, 1024, WCTL>(t0, rk, rv, ws, pbase + (252 + (k3 & 3)) * 8192, 0, lane, 8, obase, mlbase ST_PASS);
    lds_wait(B_CNT + 4, 8u);
    STAMP(9);
    const int slice = 8 * x + k3;
    final_reduce<4>(ws, WS_OPB + slice * 4096, slice, p.out, 512, tag, lane);
    STAMP(10);
    STAMP_FLUSH();
    return;
  }
  const unsigned tag = __builtin_amdgcn_readfirstlane(*LP(unsigned, B_EP)) + 1u;
  {
    v4u wf[32];
    {
      const int r0 = 32768 + (8 * (c >> 2) + (c & 3)) * 256;
#pragma unroll
      for (int s = 0; s < 4; ++s) {
        int a = r0 + (((4 * s + g) ^ c) << 4);
        PIN(a);
#pragma unroll
        for (int u = 0; u < 8; ++u) wf[4 * u + s] = lds128(a + (u >> 1) * 8192 + (u & 1) * 1024);
      }
    }
    v4u f[4];
    gather<4, 0>(f, ws, lane * 16, WS_QA + hu * 4096, tag);
    STAMP(2);
    *LP(unsigned, B_QA + lane * 4) = pk(uf(f[0][0]) + uf(f[2][0]), uf(f[0][2]) + uf(f[2][2]));
    *LP(unsigned, B_QA + 256 + lane * 4) = pk(uf(f[1][0]) + uf(f[3][0]), uf(f[1][2]) + uf(f[3][2]));
    v4u qf[4];
#pragma unroll
    for (int s = 0; s < 4; ++s) qf[s] = lds128(B_QA + (c & 1) * 256 + 64 * s + 16 * g);
    f32x4 e[8];
#pragma unroll
    for (int u = 0; u < 8; ++u) e[u] = f32x4{0.f, 0.f, 0.f, 0.f};
#pragma unroll
    for (int s = 0; s < 4; ++s)
#pragma unroll
      for (int u = 0; u < 8; ++u) e[u] = mfma16(wf[4 * u + s], qf[s], e[u]);
    const int ct = (c >> 1) & 3;
    const f32x4 d0 = ct == 0 ? e[0] : ct == 1 ? e[2] : ct == 2 ? e[4] : e[6], d1 = ct == 0 ? e[1] : ct == 1 ? e[3] : ct == 2 ? e[5] : e[7];
    if (c < 8) st_sc(v4u{pk(d0[0], d0[1]), pk(d0[2], d0[3]), pk(d1[0], d1[1]), pk(d1[2], d1[3])}, ws, (c & 1) * 32768 + ct * 64 + g * 16, WS_QABS + hu * 1024 + cq * 256);
    STAMP(3);
    VMCNT(0);
    if (lane == 0) __builtin_amdgcn_raw_buffer_store_b64(v2u{tag, ~tag}, ws, WS_QFLAG + hu * FSTR + cq * 8, 0, AUX_SC1);
    STAMP(4);
    if (lane == 0) *(volatile LAS unsigned*)(size_t)(B_CNT + 20) = 1u;
  }
  for (int k = 0; k < QBDELAY; ++k) __builtin_amdgcn_s_sleep(8);
  STAMP(5);
  fetch_qb(t0, ws, WS_QB + (b * 8 + x) * 2048, tag, lane);
  STAMP(6);
  {
    const int k2 = 2 * k3 + type, h = 4 * x + (k2 >> 2), cg = k2 & 3;
    const rsrc_t ro = mk(p.wob, 4096 * 1024);
    if (PRE & 2) {
#pragma unroll
      for (int n = 0; n < 3; ++n) dma_sc(ws, lane * 16, WS_BO + h * 12288 + (3 * cg + n) * 1024, DUMMY);
    }
    v4u wo[32];
    auto ld_wo = [&](int n) { return ld_nt(ro, g * 8192 + c * 16, (h * 128 + 32 * (n >> 3) + (n & 7)) * 1024 + cg * 256); };
#pragma unroll
    for (int n = 0; n < WO1; ++n) { wo[n] = ld_wo(n); vmcnt<WCTL - 1>(); SB(); }
#pragma unroll
    for (int t = 0; t < WO1 / 8; ++t) transpose8(wo + 8 * t);
#pragma unroll
    for (int n = 0; n < WO1; ++n) PIN(wo[n]);
    STAMP(7);
    lds_wait(B_CNT + 24, WO_LATE);
    float wt[12];
    {
      const bool act = (lane & 31) < 12;
      const int mo = (lane >> 5) * 2048 + (act ? (lane & 31) : 0) * 128;
      v4u f = ld_sc(ws, mo, WS_BML + x * 4096 + (h & 3) * 16);
      SB();
      if (TWO) pace(t0, TWO);
#pragma unroll
      for (int n = WO1; n < 32; ++n) wo[n] = ld_wo(n);
      SB();
      for (unsigned spins = 0; !all_ok(tags_ok(f, tag)) && spins < SPIN_CAP; ++spins) {
        asm volatile("" ::: "memory");
        __builtin_amdgcn_s_sleep(4);
        f = ld_sc(ws, mo, WS_BML + x * 4096 + (h & 3) * 16);
      }
      const float mm = uf(f[0]), M = row_max(mm);
      const float e = act ? ex2((mm - M) * C2) : 0.f, Lt = row_sum(e * uf(f[2]));
      if ((lane & 16) == 0) *LP(float, B_WT + ((lane >> 5) * 16 + c) * 4) = e / Lt;
      asm volatile("" ::: "memory");
#pragma unroll
      for (int s = 0; s < 3; ++s) {
        const f32x4 t = *LP(f32x4, B_WT + (lane >> 5) * 64 + s * 16);
        wt[4 * s] = t[0]; wt[4 * s + 1] = t[1]; wt[4 * s + 2] = t[2]; wt[4 * s + 3] = t[3];
      }
    }
    STAMP(8);
    if (WO1 < 32) {
#pragma unroll
      for (int t = WO1 / 8; t < 4; ++t) transpose8(wo + 8 * t);
#pragma unroll
      for (int n = WO1; n < 32; ++n) PIN(wo[n]);
    }
    STAMP(11);
    lds_wait(B_CNT + 4, 7u);
    v4u v[12];
    gather<12, SL_BO>(v, ws, lane * 16, WS_BO + h * 12288, tag);
    STAMP(13);
    float o[4] = {0.f, 0.f, 0.f, 0.f};
#pragma unroll
    for (int s = 0; s < 12; ++s) {
      o[0] = fmaf(wt[s], bflo(v[s][0]), o[0]); o[1] = fmaf(wt[s], bfhi(v[s][0]), o[1]);
      o[2] = fmaf(wt[s], bflo(v[s][2]), o[2]); o[3] = fmaf(wt[s], bfhi(v[s][2]), o[3]);
    }
    STAMP(9);
    *LP(v2u, B_QA + lane * 8) = v2u{pk(o[0], o[1]), pk(o[2], o[3])};
    asm volatile("" ::: "memory");
    out_gemv<4>(wo, B_QA, ws, WS_OPB + cg * 16 * 4096 + h * 128, 4096, tag, lane);
    STAMP(10);
  }
  STAMP_FLUSH();
}

__global__ void __launch_bounds__(512, 2) k_fused(Params p) {
  const int lane = threadIdx.x & 63, w = __builtin_amdgcn_readfirstlane(threadIdx.x >> 6);
  const int x = blockIdx.x & 7, i = blockIdx.x >> 3;
  if (w >= 6 && i + 32 * (7 - w) < CODE_KB) {
    const rsrc_t rcode = mk((const void*)(__builtin_amdgcn_s_getpc() & ~255ull), CODE_KB * 1024);
    dma_df(rcode, lane * 16, (i + 32 * (7 - w)) * 1024, i >= 24 ? 2048 : 71680);
  }
  if (i >= 24) role_a(p, x, i - 24, lane, w);
  else role_b(p, x, i, lane, w);
}

__global__ void k_amp(const unsigned* st, int mult) {
  const unsigned ref = st[0];
  int mn = 0x7fffffff, mx = -0x7fffffff;
  for (int i = threadIdx.x; i < 2048 * 16; i += 256) { const int b = i >> 7, w = (i >> 4) & 7, k = i & 15; const unsigned v = st[i]; if (!v) continue; const int d = (int)(v - ref);
    if (k == 0) mn = d < mn ? d : mn;
    if (k < 15) mx = d > mx ? d : mx; }
  __shared__ int a[256], c[256];
  a[threadIdx.x] = mn; c[threadIdx.x] = mx; __syncthreads();
  if (threadIdx.x == 0) { for (int i = 0; i < 256; ++i) { mn = a[i] < mn ? a[i] : mn; const int d = c[i]; mx = d > mx ? d : mx; }
    unsigned long long dd = (unsigned long long)(mx - mn) * mult; if (dd > 200000) dd = 200000;
    unsigned long long t0 = __builtin_amdgcn_s_memrealtime();
    while (__builtin_amdgcn_s_memrealtime() - t0 < dd) __builtin_amdgcn_s_sleep(1); }
}
extern "C" void kernel_launch(void* const* d_in, const int* in_sizes, int n_in, void* d_out, int out_size, void* d_ws, size_t ws_size, hipStream_t stream) {
  Params p;
  p.xa = d_in[0]; p.ckv = d_in[1]; p.wqa = d_in[2]; p.wuk = d_in[3]; p.wuv = d_in[4]; p.woa = d_in[5];
  p.xb = d_in[6]; p.kc = d_in[7]; p.vc = d_in[8]; p.wqb = d_in[9]; p.wob = d_in[10];
  p.out = d_out; p.ws = d_ws;
  (void)hipFuncSetAttribute((const void*)k_fused, hipFuncAttributeMaxDynamicSharedMemorySize, 163840);
  k_fused<<<256, 512, 163840, stream>>>(p);
  k_amp<<<1, 256, 0, stream>>>((const unsigned*)((char*)d_ws + WS_ST), 0);
}
```

```cpp
#include <hip/hip_runtime.h>
#include <hip/hip_bf16.h>

#define LAS __attribute__((address_space(3)))
#define DI __device__ __forceinline__
typedef unsigned v4u __attribute__((ext_vector_type(4)));
typedef unsigned v2u __attribute__((ext_vector_type(2)));
typedef float f32x4 __attribute__((ext_vector_type(4)));
typedef float f32x2 __attribute__((ext_vector_type(2)));
typedef float f32x16 __attribute__((ext_vector_type(16)));
typedef __bf16 bf16x8 __attribute__((ext_vector_type(8)));
typedef __bf16 bf16x2 __attribute__((ext_vector_type(2)));
typedef short v4s __attribute__((ext_vector_type(4)));
typedef unsigned short u16;
typedef __amdgpu_buffer_rsrc_t rsrc_t;
typedef unsigned lds_t;
#define LP(T, a) ((LAS T*)(size_t)(unsigned)(a))

constexpr int AUX_NT = 2, AUX_SC1 = 16;
constexpr float C2 = 0.088388347648318440f * 1.4426950408889634f;
constexpr unsigned SPIN_CAP = 1u << 19, LSPIN = 1u << 22;

constexpr int WS_EPOCH = 0;
constexpr int WS_QFLAG = 16384;
constexpr int WS_QA = 1572864;
constexpr int WS_QB = 65536;
constexpr int WS_AML = 98304;
constexpr int WS_QABS = 131072;
constexpr int WS_BML = 196608;
constexpr int WS_OPB = 262144;
constexpr int WS_OPA = 524288;
constexpr int WS_BO = 1048576;
constexpr int WS_AO = 2097152;
constexpr int WS_BYTES = 4194304;

struct Params {
  const void *xa, *ckv, *wqa, *wuk, *wuv, *woa, *xb, *kc, *vc, *wqb, *wob;
  void *out, *ws;
};

DI rsrc_t mk(const void* p, int bytes) { return __builtin_amdgcn_make_buffer_rsrc((void*)p, (short)0, bytes, 0x00020000); }
DI v4u ld_nt(rsrc_t r, int voff, int soff) { return __builtin_amdgcn_raw_buffer_load_b128(r, voff, soff, AUX_NT); }
DI v4u ld_sc(rsrc_t r, int voff, int soff) { return __builtin_amdgcn_raw_buffer_load_b128(r, voff, soff, AUX_SC1); }
DI void st_sc(v4u v, rsrc_t r, int voff, int soff) { __builtin_amdgcn_raw_buffer_store_b128(v, r, voff + soff, 0, AUX_SC1); }
DI void dma_nt(rsrc_t r, int voff, int soff, unsigned lds) {
  asm volatile("s_mov_b32 m0, %0\n\ts_nop 0\n\tbuffer_load_dwordx4 %1, %2, %3 offen nt lds" ::"s"(lds), "v"(voff), "s"(r), "s"(soff) : "memory");
}
DI void dma_df(rsrc_t r, int voff, int soff, unsigned lds) {
  asm volatile("s_mov_b32 m0, %0\n\ts_nop 0\n\tbuffer_load_dwordx4 %1, %2, %3 offen lds" ::"s"(lds), "v"(voff), "s"(r), "s"(soff) : "memory");
}
DI void dma_sc(rsrc_t r, int voff, int soff, unsigned lds) {
  asm volatile("s_mov_b32 m0, %0\n\ts_nop 0\n\tbuffer_load_dwordx4 %1, %2, %3 offen sc1 lds" ::"s"(lds), "v"(voff), "s"(r), "s"(soff) : "memory");
}
#define VMCNT(n) asm volatile("s_waitcnt vmcnt(" #n ")" ::: "memory")
template <int N> DI void vmcnt() { asm volatile("s_waitcnt vmcnt(%0)" ::"i"(N) : "memory"); }
#define SB() __builtin_amdgcn_sched_barrier(0)
#define PIN(x) asm volatile("" : "+v"(x))
#define W1 6
#define PFN 8
#define PFWV 1
#define WQK 8
#define WCTL 5
#define WBETA 7
#define WO_EARLY 8
#define WV_S 5
#define FSTR 32
#define QDELAY 0
#define ILV 1
#define QBDELAY 0
#define PA 0
#define PB 0
#define PSTEP 4
#define WO1 0
#define WO_LATE 7
#define QBT 700
#define SL_AML 8
#define D_AML 0
#define SL_BO 0
#define SL_FA 0
#define D_FA 30
#define PRE 3
#define GI 2
#define TWO 0
#define TWA 0
#define OUTC 1
#define OUTAUX 0
#define OUTPRE 0
#define HOLD 0
#define CODE_KB 33
DI void bar() { asm volatile("s_waitcnt lgkmcnt(0)\n\ts_barrier" ::: "memory"); }
DI float bflo(unsigned u) { return __builtin_bit_cast(float, u << 16); }
DI float bfhi(unsigned u) { return __builtin_bit_cast(float, u & 0xffff0000u); }
DI unsigned fu(float f) { return __builtin_bit_cast(unsigned, f); }
DI float uf(unsigned u) { return __builtin_bit_cast(float, u); }
DI unsigned pk(float lo, float hi) { f32x2 v = {lo, hi}; return __builtin_bit_cast(unsigned, __builtin_convertvector(v, bf16x2)); }
DI float ex2(float x) { return __builtin_amdgcn_exp2f(x); }
DI f32x4 mfma16(v4u a, v4u b, f32x4 c) { return __builtin_amdgcn_mfma_f32_16x16x32_bf16(__builtin_bit_cast(bf16x8, a), __builtin_bit_cast(bf16x8, b), c, 0, 0, 0); }
DI f32x16 mfma32(v4u a, v4u b, f32x16 c) { return __builtin_amdgcn_mfma_f32_32x32x16_bf16(__builtin_bit_cast(bf16x8, a), __builtin_bit_cast(bf16x8, b), c, 0, 0, 0); }
DI v4u lds128(lds_t p) { return *LP(v4u, p); }
DI v2u ldstr(lds_t p) { return __builtin_bit_cast(v2u, __builtin_amdgcn_ds_read_tr16_b64_v4i16(LP(v4s, p))); }
DI float x16_max(float x) { auto s = __builtin_amdgcn_permlane16_swap(fu(x), fu(x), false, false); return fmaxf(uf(s[0]), uf(s[1])); }
DI float x32_max(float x) { auto s = __builtin_amdgcn_permlane32_swap(fu(x), fu(x), false, false); return fmaxf(uf(s[0]), uf(s[1])); }
DI float x16_sum(float x) { auto s = __builtin_amdgcn_permlane16_swap(fu(x), fu(x), false, false); return uf(s[0]) + uf(s[1]); }
DI float x32_sum(float x) { auto s = __builtin_amdgcn_permlane32_swap(fu(x), fu(x), false, false); return uf(s[0]) + uf(s[1]); }
template <int CTRL> DI float dpp(float x) { return uf(__builtin_amdgcn_mov_dpp(fu(x), CTRL, 0xf, 0xf, true)); }
constexpr int XOR1 = 0xB1, XOR2 = 0x4E, ROR4 = 0x124, ROR8 = 0x128;
DI float row_max(float x) { x = fmaxf(x, dpp<XOR1>(x)); x = fmaxf(x, dpp<XOR2>(x)); x = fmaxf(x, dpp<ROR4>(x)); return fmaxf(x, dpp<ROR8>(x)); }
DI float row_sum(float x) { x += dpp<XOR1>(x); x += dpp<XOR2>(x); x += dpp<ROR4>(x); return x + dpp<ROR8>(x); }
DI unsigned fsw(unsigned row) { return ((row & 3) << 2) | ((row >> 2) & 3); }
DI bool tags_ok(v4u v, unsigned tag) { return v[1] == tag && v[3] == tag; }
DI bool all_ok(bool ok) { return __builtin_amdgcn_ballot_w64(!ok) == 0ull; }
DI v4u tfrag(const v4u* r, int i) {
  v4u o;
#pragma unroll
  for (int t = 0; t < 4; ++t) o[t] = __builtin_amdgcn_perm(r[2 * t + 1][i >> 1], r[2 * t][i >> 1], (i & 1) ? 0x07060302u : 0x05040100u);
  return o;
}
DI void transpose8(v4u* r) {
  v4u o[8];
#pragma unroll
  for (int i = 0; i < 8; ++i) o[i] = tfrag(r, i);
#pragma unroll
  for (int i = 0; i < 8; ++i) r[i] = o[i];
}

#define STAMP(i)
#define STAMP_DECL
#define STAMP_FLUSH()

template <int N, int SLEEP> DI void gather(v4u* v, rsrc_t r, int voff, int soff, unsigned tag) {
  unsigned a, t, u, cnt;
  if constexpr (N == 1) asm volatile(
      "s_mov_b32 %[cnt], %[cap]\n"
      "buffer_load_dwordx4 v[252:255], %[vo], %[rs], %[s0] offen offset:0 sc1\n"
      ".Lg_loop%=:\n"
      "s_waitcnt vmcnt(0)\n"
      "v_bitop3_b32 %[a], v253, v255, %[tag] bitop3:0x7e\n"
      "v_cmp_ne_u32 vcc, 0, %[a]\n"
      "s_cbranch_vccz .Lg_done%=\n"
      "s_sub_u32 %[cnt], %[cnt], 1\n"
      "s_cbranch_scc1 .Lg_done%=\n"
      "s_sleep %[sl]\n"
      "s_mov_b64 exec, vcc\n"
      "buffer_load_dwordx4 v[252:255], %[vo], %[rs], %[s0] offen offset:0 sc1\n"
      "s_mov_b64 exec, -1\n"
      "s_branch .Lg_loop%=\n"
      ".Lg_done%=:\n"
      : "=&{v[252:255]}"(v[0]), [a] "=&v"(a), [t] "=&v"(t), [u] "=&v"(u), [cnt] "=&s"(cnt)
      : [vo] "v"(voff), [rs] "s"(r), [tag] "s"(tag), [s0] "s"(soff + 0), [cap] "i"(SPIN_CAP), [sl] "i"(SLEEP) : "vcc", "scc", "memory");
  if constexpr (N == 2) asm volatile(
      "s_mov_b32 %[cnt], %[cap]\n"
      "buffer_load_dwordx4 v[248:251], %[vo], %[rs], %[s0] offen offset:0 sc1\n"
      "buffer_load_dwordx4 v[252:255], %[vo], %[rs], %[s0] offen offset:1024 sc1\n"
      ".Lg_loop%=:\n"
      "s_waitcnt vmcnt(0)\n"
      "v_bitop3_b32 %[a], v249, v251, %[tag] bitop3:0x7e\n"
      "v_bitop3_b32 %[t], v253, v255, %[tag] bitop3:0x7e\n"
      "v_or_b32 %[a], %[a], %[t]\n"
      "v_cmp_ne_u32 vcc, 0, %[a]\n"
      "s_cbranch_vccz .Lg_done%=\n"
      "s_sub_u32 %[cnt], %[cnt], 1\n"
      "s_cbranch_scc1 .Lg_done%=\n"
      "s_sleep %[sl]\n"
      "v_bitop3_b32 %[t], v249, v251, %[tag] bitop3:0x7e\n"
      "v_cmp_ne_u32 vcc, 0, %[t]\n"
      "s_mov_b64 exec, vcc\n"
      "buffer_load_dwordx4 v[248:251], %[vo], %[rs], %[s0] offen offset:0 sc1\n"
      "s_mov_b64 exec, -1\n"
      "v_bitop3_b32 %[t], v253, v255, %[tag] bitop3:0x7e\n"
      "v_cmp_ne_u32 vcc, 0, %[t]\n"
      "s_mov_b64 exec, vcc\n"
      "buffer_load_dwordx4 v[252:255], %[vo], %[rs], %[s0] offen offset:1024 sc1\n"
      "s_mov_b64 exec, -1\n"
      "s_branch .Lg_loop%=\n"
      ".Lg_done%=:\n"
      : "=&{v[248:251]}"(v[0]), "=&{v[252:255]}"(v[1]), [a] "=&v"(a), [t] "=&v"(t), [u] "=&v"(u), [cnt] "=&s"(cnt)
      : [vo] "v"(voff), [rs] "s"(r), [tag] "s"(tag), [s0] "s"(soff + 0), [cap] "i"(SPIN_CAP), [sl] "i"(SLEEP) : "vcc", "scc", "memory");
  if constexpr (N == 4) asm volatile(
      "s_mov_b32 %[cnt], %[cap]\n"
      "buffer_load_dwordx4 v[240:243], %[vo], %[rs], %[s0] offen offset:0 sc1\n"
      "buffer_load_dwordx4 v[244:247], %[vo], %[rs], %[s0] offen offset:1024 sc1\n"
      "buffer_load_dwordx4 v[248:251], %[vo], %[rs], %[s0] offen offset:2048 sc1\n"
      "buffer_load_dwordx4 v[252:255], %[vo], %[rs], %[s0] offen offset:3072 sc1\n"
      ".Lg_loop%=:\n"
      "s_waitcnt vmcnt(0)\n"
      "v_bitop3_b32 %[a], v241, v243, %[tag] bitop3:0x7e\n"
      "v_bitop3_b32 %[t], v245, v247, %[tag] bitop3:0x7e\n"
      "v_bitop3_b32 %[u], v249, v251, %[tag] bitop3:0x7e\n"
      "v_or3_b32 %[a], %[a], %[t], %[u]\n"
      "v_bitop3_b32 %[t], v253, v255, %[tag] bitop3:0x7e\n"
      "v_or_b32 %[a], %[a], %[t]\n"
      "v_cmp_ne_u32 vcc, 0, %[a]\n"
      "s_cbranch_vccz .Lg_done%=\n"
      "s_sub_u32 %[cnt], %[cnt], 1\n"
      "s_cbranch_scc1 .Lg_done%=\n"
      "s_sleep %[sl]\n"
      "v_bitop3_b32 %[t], v241, v243, %[tag] bitop3:0x7e\n"
      "v_cmp_ne_u32 vcc, 0, %[t]\n"
      "s_mov_b64 exec, vcc\n"
      "buffer_load_dwordx4 v[240:243], %[vo], %[rs], %[s0] offen offset:0 sc1\n"
      "s_mov_b64 exec, -1\n"
      "v_bitop3_b32 %[t], v245, v247, %[tag] bitop3:0x7e\n"
      "v_cmp_ne_u32 vcc, 0, %[t]\n"
      "s_mov_b64 exec, vcc\n"
      "buffer_load_dwordx4 v[244:247], %[vo], %[rs], %[s0] offen offset:1024 sc1\n"
      "s_mov_b64 exec, -1\n"
      "v_bitop3_b32 %[t], v249, v251, %[tag] bitop3:0x7e\n"
      "v_cmp_ne_u32 vcc, 0, %[t]\n"
      "s_mov_b64 exec, vcc\n"
      "buffer_load_dwordx4 v[248:251], %[vo], %[rs], %[s0] offen offset:2048 sc1\n"
      "s_mov_b64 exec, -1\n"
      "v_bitop3_b32 %[t], v253, v255, %[tag] bitop3:0x7e\n"
      "v_cmp_ne_u32 vcc, 0, %[t]\n"
      "s_mov_b64 exec, vcc\n"
      "buffer_load_dwordx4 v[252:255], %[vo], %[rs], %[s0] offen offset:3072 sc1\n"
      "s_mov_b64 exec, -1\n"
      "s_branch .Lg_loop%=\n"
      ".Lg_done%=:\n"
      : "=&{v[240:243]}"(v[0]), "=&{v[244:247]}"(v[1]), "=&{v[248:251]}"(v[2]), "=&{v[252:255]}"(v[3]), [a] "=&v"(a), [t] "=&v"(t), [u] "=&v"(u), [cnt] "=&s"(cnt)
      : [vo] "v"(voff), [rs] "s"(r), [tag] "s"(tag), [s0] "s"(soff + 0), [cap] "i"(SPIN_CAP), [sl] "i"(SLEEP) : "vcc", "scc", "memory");
  if constexpr (N == 8) asm volatile(
      "s_mov_b32 %[cnt], %[cap]\n"
      "buffer_load_dwordx4 v[224:227], %[vo], %[rs], %[s0] offen offset:0 sc1\n"
      "buffer_load_dwordx4 v[228:231], %[vo], %[rs], %[s0] offen offset:1024 sc1\n"
      "buffer_load_dwordx4 v[232:235], %[vo], %[rs], %[s0] offen offset:2048 sc1\n"
      "buffer_load_dwordx4 v[236:239], %[vo], %[rs], %[s0] offen offset:3072 sc1\n"
      "buffer_load_dwordx4 v[240:243], %[vo], %[rs], %[s1] offen offset:0 sc1\n"
      "buffer_load_dwordx4 v[244:247], %[vo], %[rs], %[s1] offen offset:1024 sc1\n"
      "buffer_load_dwordx4 v[248:251], %[vo], %[rs], %[s1] offen offset:2048 sc1\n"
      "buffer_load_dwordx4 v[252:255], %[vo], %[rs], %[s1] offen offset:3072 sc1\n"
      ".Lg_loop%=:\n"
      "s_waitcnt vmcnt(0)\n"
      "v_bitop3_b32 %[a], v225, v227, %[tag] bitop3:0x7e\n"
      "v_bitop3_b32 %[t], v229, v231, %[tag] bitop3:0x7e\n"
      "v_bitop3_b32 %[u], v233, v235, %[tag] bitop3:0x7e\n"
      "v_or3_b32 %[a], %[a], %[t], %[u]\n"
      "v_bitop3_b32 %[t], v237, v239, %[tag] bitop3:0x7e\n"
      "v_bitop3_b32 %[u], v241, v243, %[tag] bitop3:0x7e\n"
      "v_or3_b32 %[a], %[a], %[t], %[u]\n"
      "v_bitop3_b32 %[t], v245, v247, %[tag] bitop3:0x7e\n"
      "v_bitop3_b32 %[u], v249, v251, %[tag] bitop3:0x7e\n"
      "v_or3_b32 %[a], %[a], %[t], %[u]\n"
      "v_bitop3_b32 %[t], v253, v255, %[tag] bitop3:0x7e\n"
      "v_or_b32 %[a], %[a], %[t]\n"
      "v_cmp_ne_u32 vcc, 0, %[a]\n"
      "s_cbranch_vccz .Lg_done%=\n"
      "s_sub_u32 %[cnt], %[cnt], 1\n"
      "s_cbranch_scc1 .Lg_done%=\n"
      "s_sleep %[sl]\n"
      "v_bitop3_b32 %[t], v225, v227, %[tag] bitop3:0x7e\n"
      "v_cmp_ne_u32 vcc, 0, %[t]\n"
      "s_mov_b64 exec, vcc\n"
      "buffer_load_dwordx4 v[224:227], %[vo], %[rs], %[s0] offen offset:0 sc1\n"
      "s_mov_b64 exec, -1\n"
      "v_bitop3_b32 %[t], v229, v231, %[tag] bitop3:0x7e\n"
      "v_cmp_ne_u32 vcc, 0, %[t]\n"
      "s_mov_b64 exec, vcc\n"
      "buffer_load_dwordx4 v[228:231], %[vo], %[rs], %[s0] offen offset:1024 sc1\n"
      "s_mov_b64 exec, -1\n"
      "v_bitop3_b32 %[t], v233, v235, %[tag] bitop3:0x7e\n"
      "v_cmp_ne_u32 vcc, 0, %[t]\n"
      "s_mov_b64 exec, vcc\n"
      "buffer_load_dwordx4 v[232:235], %[vo], %[rs], %[s0] offen offset:2048 sc1\n"
      "s_mov_b64 exec, -1\n"
      "v_bitop3_b32 %[t], v237, v239, %[tag] bitop3:0x7e\n"
      "v_cmp_ne_u32 vcc, 0, %[t]\n"
      "s_mov_b64 exec, vcc\n"
      "buffer_load_dwordx4 v[236:239], %[vo], %[rs], %[s0] offen offset:3072 sc1\n"
      "s_mov_b64 exec, -1\n"
      "v_bitop3_b32 %[t], v241, v243, %[tag] bitop3:0x7e\n"
      "v_cmp_ne_u32 vcc, 0, %[t]\n"
      "s_mov_b64 exec, vcc\n"
      "buffer_load_dwordx4 v[240:243], %[vo], %[rs], %[s1] offen offset:0 sc1\n"
      "s_mov_b64 exec, -1\n"
      "v_bitop3_b32 %[t], v245, v247, %[tag] bitop3:0x7e\n"
      "v_cmp_ne_u32 vcc, 0, %[t]\n"
      "s_mov_b64 exec, vcc\n"
      "buffer_load_dwordx4 v[244:247], %[vo], %[rs], %[s1] offen offset:1024 sc1\n"
      "s_mov_b64 exec, -1\n"
      "v_bitop3_b32 %[t], v249, v251, %[tag] bitop3:0x7e\n"
      "v_cmp_ne_u32 vcc, 0, %[t]\n"
      "s_mov_b64 exec, vcc\n"
      "buffer_load_dwordx4 v[248:251], %[vo], %[rs], %[s1] offen offset:2048 sc1\n"
      "s_mov_b64 exec, -1\n"
      "v_bitop3_b32 %[t], v253, v255, %[tag] bitop3:0x7e\n"
      "v_cmp_ne_u32 vcc, 0, %[t]\n"
      "s_mov_b64 exec, vcc\n"
      "buffer_load_dwordx4 v[252:255], %[vo], %[rs], %[s1] offen offset:3072 sc1\n"
      "s_mov_b64 exec, -1\n"
      "s_branch .Lg_loop%=\n"
      ".Lg_done%=:\n"
      : "=&{v[224:227]}"(v[0]), "=&{v[228:231]}"(v[1]), "=&{v[232:235]}"(v[2]), "=&{v[236:239]}"(v[3]), "=&{v[240:243]}"(v[4]), "=&{v[244:247]}"(v[5]), "=&{v[248:251]}"(v[6]), "=&{v[252:255]}"(v[7]), [a] "=&v"(a), [t] "=&v"(t), [u] "=&v"(u), [cnt] "=&s"(cnt)
      : [vo] "v"(voff), [rs] "s"(r), [tag] "s"(tag), [s0] "s"(soff + 0), [s1] "s"(soff + 4096), [cap] "i"(SPIN_CAP), [sl] "i"(SLEEP) : "vcc", "scc", "memory");
  if constexpr (N == 12) asm volatile(
      "s_mov_b32 %[cnt], %[cap]\n"
      "buffer_load_dwordx4 v[208:211], %[vo], %[rs], %[s0] offen offset:0 sc1\n"
      "buffer_load_dwordx4 v[212:215], %[vo], %[rs], %[s0] offen offset:1024 sc1\n"
      "buffer_load_dwordx4 v[216:219], %[vo], %[rs], %[s0] offen offset:2048 sc1\n"
      "buffer_load_dwordx4 v[220:223], %[vo], %[rs], %[s0] offen offset:3072 sc1\n"
      "buffer_load_dwordx4 v[224:227], %[vo], %[rs], %[s1] offen offset:0 sc1\n"
      "buffer_load_dwordx4 v[228:231], %[vo], %[rs], %[s1] offen offset:1024 sc1\n"
      "buffer_load_dwordx4 v[232:235], %[vo], %[rs], %[s1] offen offset:2048 sc1\n"
      "buffer_load_dwordx4 v[236:239], %[vo], %[rs], %[s1] offen offset:3072 sc1\n"
      "buffer_load_dwordx4 v[240:243], %[vo], %[rs], %[s2] offen offset:0 sc1\n"
      "buffer_load_dwordx4 v[244:247], %[vo], %[rs], %[s2] offen offset:1024 sc1\n"
      "buffer_load_dwordx4 v[248:251], %[vo], %[rs], %[s2] offen offset:2048 sc1\n"
      "buffer_load_dwordx4 v[252:255], %[vo], %[rs], %[s2] offen offset:3072 sc1\n"
      ".Lg_loop%=:\n"
      "s_waitcnt vmcnt(0)\n"
      "v_bitop3_b32 %[a], v209, v211, %[tag] bitop3:0x7e\n"
      "v_bitop3_b32 %[t], v213, v215, %[tag] bitop3:0x7e\n"
      "v_bitop3_b32 %[u], v217, v219, %[tag] bitop3:0x7e\n"
      "v_or3_b32 %[a], %[a], %[t], %[u]\n"
      "v_bitop3_b32 %[t], v221, v223, %[tag] bitop3:0x7e\n"
      "v_bitop3_b32 %[u], v225, v227, %[tag] bitop3:0x7e\n"
      "v_or3_b32 %[a], %[a], %[t], %[u]\n"
      "v_bitop3_b32 %[t], v229, v231, %[tag] bitop3:0x7e\n"
      "v_bitop3_b32 %[u], v233, v235, %[tag] bitop3:0x7e\n"
      "v_or3_b32 %[a], %[a], %[t], %[u]\n"
      "v_bitop3_b32 %[t], v237, v239, %[tag] bitop3:0x7e\n"
      "v_bitop3_b32 %[u], v241, v243, %[tag] bitop3:0x7e\n"
      "v_or3_b32 %[a], %[a], %[t], %[u]\n"
      "v_bitop3_b32 %[t], v245, v247, %[tag] bitop3:0x7e\n"
      "v_bitop3_b32 %[u], v249, v251, %[tag] bitop3:0x7e\n"
      "v_or3_b32 %[a], %[a], %[t], %[u]\n"
      "v_bitop3_b32 %[t], v253, v255, %[tag] bitop3:0x7e\n"
      "v_or_b32 %[a], %[a], %[t]\n"
      "v_cmp_ne_u32 vcc, 0, %[a]\n"
      "s_cbranch_vccz .Lg_done%=\n"
      "s_sub_u32 %[cnt], %[cnt], 1\n"
      "s_cbranch_scc1 .Lg_done%=\n"
      "s_sleep %[sl]\n"
      "v_bitop3_b32 %[t], v209, v211, %[tag] bitop3:0x7e\n"
      "v_cmp_ne_u32 vcc, 0, %[t]\n"
      "s_mov_b64 exec, vcc\n"
      "buffer_load_dwordx4 v[208:211], %[vo], %[rs], %[s0] offen offset:0 sc1\n"
      "s_mov_b64 exec, -1\n"
      "v_bitop3_b32 %[t], v213, v215, %[tag] bitop3:0x7e\n"
      "v_cmp_ne_u32 vcc, 0, %[t]\n"
      "s_mov_b64 exec, vcc\n"
      "buffer_load_dwordx4 v[212:215], %[vo], %[rs], %[s0] offen offset:1024 sc1\n"
      "s_mov_b64 exec, -1\n"
      "v_bitop3_b32 %[t], v217, v219, %[tag] bitop3:0x7e\n"
      "v_cmp_ne_u32 vcc, 0, %[t]\n"
      "s_mov_b64 exec, vcc\n"
      "buffer_load_dwordx4 v[216:219], %[vo], %[rs], %[s0] offen offset:2048 sc1\n"
      "s_mov_b64 exec, -1\n"
      "v_bitop3_b32 %[t], v221, v223, %[tag] bitop3:0x7e\n"
      "v_cmp_ne_u32 vcc, 0, %[t]\n"
      "s_mov_b64 exec, vcc\n"
      "buffer_load_dwordx4 v[220:223], %[vo], %[rs], %[s0] offen offset:3072 sc1\n"
      "s_mov_b64 exec, -1\n"
      "v_bitop3_b32 %[t], v225, v227, %[tag] bitop3:0x7e\n"
      "v_cmp_ne_u32 vcc, 0, %[t]\n"
      "s_mov_b64 exec, vcc\n"
      "buffer_load_dwordx4 v[224:227], %[vo], %[rs], %[s1] offen offset:0 sc1\n"
      "s_mov_b64 exec, -1\n"
      "v_bitop3_b32 %[t], v229, v231, %[tag] bitop3:0x7e\n"
      "v_cmp_ne_u32 vcc, 0, %[t]\n"
      "s_mov_b64 exec, vcc\n"
      "buffer_load_dwordx4 v[228:231], %[vo], %[rs], %[s1] offen offset:1024 sc1\n"
      "s_mov_b64 exec, -1\n"
      "v_bitop3_b32 %[t], v233, v235, %[tag] bitop3:0x7e\n"
      "v_cmp_ne_u32 vcc, 0, %[t]\n"
      "s_mov_b64 exec, vcc\n"
      "buffer_load_dwordx4 v[232:235], %[vo], %[rs], %[s1] offen offset:2048 sc1\n"
      "s_mov_b64 exec, -1\n"
      "v_bitop3_b32 %[t], v237, v239, %[tag] bitop3:0x7e\n"
      "v_cmp_ne_u32 vcc, 0, %[t]\n"
      "s_mov_b64 exec, vcc\n"
      "buffer_load_dwordx4 v[236:239], %[vo], %[rs], %[s1] offen offset:3072 sc1\n"
      "s_mov_b64 exec, -1\n"
      "v_bitop3_b32 %[t], v241, v243, %[tag] bitop3:0x7e\n"
      "v_cmp_ne_u32 vcc, 0, %[t]\n"
      "s_mov_b64 exec, vcc\n"
      "buffer_load_dwordx4 v[240:243], %[vo], %[rs], %[s2] offen offset:0 sc1\n"
      "s_mov_b64 exec, -1\n"
      "v_bitop3_b32 %[t], v245, v247, %[tag] bitop3:0x7e\n"
      "v_cmp_ne_u32 vcc, 0, %[t]\n"
      "s_mov_b64 exec, vcc\n"
      "buffer_load_dwordx4 v[244:247], %[vo], %[rs], %[s2] offen offset:1024 sc1\n"
      "s_mov_b64 exec, -1\n"
      "v_bitop3_b32 %[t], v249, v251, %[tag] bitop3:0x7e\n"
      "v_cmp_ne_u32 vcc, 0, %[t]\n"
      "s_mov_b64 exec, vcc\n"
      "buffer_load_dwordx4 v[248:251], %[vo], %[rs], %[s2] offen offset:2048 sc1\n"
      "s_mov_b64 exec, -1\n"
      "v_bitop3_b32 %[t], v253, v255, %[tag] bitop3:0x7e\n"
      "v_cmp_ne_u32 vcc, 0, %[t]\n"
      "s_mov_b64 exec, vcc\n"
      "buffer_load_dwordx4 v[252:255], %[vo], %[rs], %[s2] offen offset:3072 sc1\n"
      "s_mov_b64 exec, -1\n"
      "s_branch .Lg_loop%=\n"
      ".Lg_done%=:\n"
      : "=&{v[208:211]}"(v[0]), "=&{v[212:215]}"(v[1]), "=&{v[216:219]}"(v[2]), "=&{v[220:223]}"(v[3]), "=&{v[224:227]}"(v[4]), "=&{v[228:231]}"(v[5]), "=&{v[232:235]}"(v[6]), "=&{v[236:239]}"(v[7]), "=&{v[240:243]}"(v[8]), "=&{v[244:247]}"(v[9]), "=&{v[248:251]}"(v[10]), "=&{v[252:255]}"(v[11]), [a] "=&v"(a), [t] "=&v"(t), [u] "=&v"(u), [cnt] "=&s"(cnt)
      : [vo] "v"(voff), [rs] "s"(r), [tag] "s"(tag), [s0] "s"(soff + 0), [s1] "s"(soff + 4096), [s2] "s"(soff + 8192), [cap] "i"(SPIN_CAP), [sl] "i"(SLEEP) : "vcc", "scc", "memory");
}
DI unsigned now() { return (unsigned)__builtin_amdgcn_s_memrealtime(); }
DI void pace(unsigned t0, unsigned dt) { while (now() - t0 < dt) __builtin_amdgcn_s_sleep(1); }
template <int KS> DI void out_gemv(const v4u* wf, lds_t ystage, rsrc_t ws, int dst, int slice_stride, unsigned tag, int lane) {
  const int c = lane & 15, g = lane >> 4;
  v4u a[KS];
#pragma unroll
  for (int t = 0; t < KS; ++t) a[t] = lds128(ystage + ((OUTC ? c : c >> 2) & 1) * (64 * KS) + 64 * t + 16 * g);
  f32x4 d[8];
#pragma unroll
  for (int i = 0; i < 8; ++i) d[i] = f32x4{0.f, 0.f, 0.f, 0.f};
#pragma unroll
  for (int t = 0; t < KS; ++t)
#pragma unroll
    for (int i = 0; i < 8; ++i) d[i] = mfma16(a[t], wf[8 * t + i], d[i]);
  float v[4];
#pragma unroll
  for (int k = 0; k < 4; ++k) { const int e = k >> 1, o = k & 1; const float lo = (g & 1) ? d[2 + o][e] : d[o][e], hi = (g & 1) ? d[6 + o][e] : d[4 + o][e]; v[k] = g >= 2 ? hi : lo; }
  const int voff = (c >> 1) * slice_stride + (4 * (c & 1) + g) * 16;
  st_sc(v4u{fu(v[0]), tag, fu(v[1]), tag}, ws, voff, dst);
  st_sc(v4u{fu(v[2]), tag, fu(v[3]), tag}, ws, voff, dst + 8 * slice_stride);
}
template <int NL> DI void final_reduce(rsrc_t ws, int src, int slice, void* out, int colbase, unsigned tag, int lane) {
  const rsrc_t ro = mk(out, 4096);
  int oo = (((slice >> 3) & 1) * 1024 + colbase + 128 * (slice >> 4) + 16 * (slice & 7) + 2 * (lane & 7)) * 2;
  PIN(oo);
  if (OUTPRE) dma_sc(ro, oo & ~15, 0, NL == 8 ? 24576 : 71680);
  v4u v[NL];
  if (NL == 8 && D_FA) __builtin_amdgcn_s_sleep(D_FA);
  gather<NL, NL == 8 ? SL_FA : 0>(v, ws, lane * 16, src, tag);
  float s0 = 0.f, s1 = 0.f;
#pragma unroll
  for (int i = 0; i < NL; ++i) { s0 += uf(v[i][0]); s1 += uf(v[i][2]); }
  s0 += dpp<ROR8>(s0); s1 += dpp<ROR8>(s1);
  s0 = x32_sum(x16_sum(s0)); s1 = x32_sum(x16_sum(s1));
  if (lane < 8) __builtin_amdgcn_raw_buffer_store_b32(pk(s0, s1), ro, oo, 0, OUTAUX);
}

DI void role_a(const Params& p, int x, int i, int lane, int w) {
  STAMP_DECL
  STAMP(0);
  const unsigned t0 = now();
  const int b = x & 1, sp = (x >> 1) * 8 + i, h = 4 * ((i & 6) + (x & 1)) + (((x >> 1) + (i >> 1)) & 3), dh = i & 1;
  const int c = lane & 15, g = lane >> 4;
  const rsrc_t ws = mk(p.ws, WS_BYTES), rc = mk(p.ckv, 2 * 8192 * 1024);
  constexpr int IMG = 32768;
  const int kbase = (b * 8192 + sp * 256) * 1024;
  auto krow = [&](int row) { return kbase + ((row + 32 * i) & 255) * 1024; };
  if (w == 0) dma_sc(ws, 0, WS_EPOCH, 1024);
  {
    const int v0 = (lane ^ fsw(2 * w)) << 4, v1 = (lane ^ fsw(2 * w + 1)) << 4;
#pragma unroll
    for (int j = 0; j < 16; ++j) { const int row = 2 * w + (j & 1) + 16 * (j >> 1); dma_nt(rc, (j & 1) ? v1 : v0, krow(row), IMG + row * 1024); }
  }
  int ts = c * 1024 + 256 * g + fsw(c) * 16;
  const int hh = lane >> 5, blk = (lane >> 4) & 1, q4 = (lane & 15) >> 2, p4 = lane & 3;
  int ta[2][2];
#pragma unroll
  for (int ct = 0; ct < 2; ++ct)
#pragma unroll
    for (int t = 0; t < 2; ++t) { const int row = 8 * hh + 4 * t + q4; ta[ct][t] = IMG + row * 1024 + (((8 * w + 4 * ct + 2 * blk + (p4 >> 1)) ^ fsw(row)) << 4) + 8 * (p4 & 1); PIN(ta[ct][t]); }
  auto trfrag = [&](int ct, int ks) { const v2u r0 = ldstr(ta[ct][0] + ks * 16384), r1 = ldstr(ta[ct][1] + ks * 16384); return v4u{r0[0], r0[1], r1[0], r1[1]}; };
  int vv[4];
#pragma unroll
  for (int j = 0; j < 4; ++j) vv[j] = (lane ^ fsw(4 * (w & 3) + j)) << 4;
  v4u sa[16], pa[8][2];
  VMCNT(8);
  bar();
  STAMP(1);
  const unsigned tag = __builtin_amdgcn_readfirstlane(*LP(unsigned, 1024)) + 1u;
  if (w < 4) {
#pragma unroll
    for (int s = 0; s < 16; ++s) sa[s] = lds128(IMG + w * 16384 + (ts ^ (s << 4)));
#pragma unroll
    for (int s = 0; s < 16; ++s) PIN(sa[s]);
  }
#pragma unroll
  for (int ks = 0; ks < 4; ++ks) { pa[ks][0] = trfrag(0, ks); pa[ks][1] = trfrag(1, ks); }
#pragma unroll
  for (int ks = 0; ks < 4; ++ks) { PIN(pa[ks][0]); PIN(pa[ks][1]); }
  bar();
  if (w >= 4) {
#pragma unroll
    for (int j = 0; j < 16; ++j) { const int row = 4 * (w - 4) + (j & 3) + 16 * (j >> 2); dma_nt(rc, vv[j & 3], krow(128 + row), IMG + row * 1024); }
    VMCNT(16);
  } else {
    VMCNT(0);
  }
  bar();
  STAMP(2);
  PIN(ts);
  if (w >= 4) {
#pragma unroll
    for (int s = 0; s < 16; ++s) sa[s] = lds128(IMG + w * 16384 + (ts ^ (s << 4)));
#pragma unroll
    for (int s = 0; s < 16; ++s) PIN(sa[s]);
  }
#pragma unroll
  for (int ks = 4; ks < 8; ++ks) { pa[ks][0] = trfrag(0, ks); pa[ks][1] = trfrag(1, ks); }
#pragma unroll
  for (int ks = 4; ks < 8; ++ks) { PIN(pa[ks][0]); PIN(pa[ks][1]); }
  bar();
  STAMP(13);
  if (w >= 4) {
#pragma unroll
    for (int j = 16; j < 32; ++j) { const int row = 4 * (w - 4) + (j & 3) + 16 * (j >> 2); vmcnt<WBETA - 1>(); dma_nt(rc, vv[j & 3], krow(128 + row), IMG + row * 1024); }
    STAMP(3);
  } else if (w == 0) {
    for (int k = 0; k < QDELAY; ++k) __builtin_amdgcn_s_sleep(8);
    for (unsigned spins = 0;; ++spins) {
      const v4u f = ld_sc(ws, (lane >> 1) * FSTR + (lane & 1) * 16, WS_QFLAG);
      if (all_ok(f[0] == tag && f[1] == ~tag && f[2] == tag && f[3] == ~tag) || spins >= SPIN_CAP) break;
      asm volatile("" ::: "memory");
      __builtin_amdgcn_s_sleep(2);
    }
    STAMP(3);
#pragma unroll
    for (int j = 0; j < 32; ++j) dma_sc(ws, (lane ^ fsw(j)) << 4, WS_QABS + b * 32768 + j * 1024, j * 1024);
  }
  VMCNT(0);
  bar();
  STAMP(4);
  PIN(ts);
  f32x4 d[2][2];
#pragma unroll
  for (int k = 0; k < 4; ++k) d[k >> 1][k & 1] = f32x4{0.f, 0.f, 0.f, 0.f};
  v4u wv[8], wo[16];
  const rsrc_t rv = mk(p.wuv, 512 * 8192);
  const int vo = (lane >> 3) * 8 * 8192 + (lane & 7) * 16;
  const rsrc_t ro = mk(p.woa, 4096 * 1024);
  auto ld_wo = [&](int n) { return ld_nt(ro, g * 8192 + c * 16, (h * 128 + dh * 64 + 32 * (n >> 3) + (n & 7)) * 1024 + w * 256); };
  {
    v4u q0[3], q1[3], sb[3];
#pragma unroll
    for (int s = 0; s < 18; ++s) {
      if (s < 16) { const int a = ts ^ (s << 4); q0[s % 3] = lds128(a); q1[s % 3] = lds128(a + 16384); sb[s % 3] = lds128(a + (IMG + w * 16384)); }
      SB();
      if (s >= 2) {
        const int t = s - 2;
        d[0][0] = mfma16(sa[t], q0[t % 3], d[0][0]); d[0][1] = mfma16(sa[t], q1[t % 3], d[0][1]);
        d[1][0] = mfma16(sb[t % 3], q0[t % 3], d[1][0]); d[1][1] = mfma16(sb[t % 3], q1[t % 3], d[1][1]);
        { const int k = t < 3 ? t : (t % 3 == 2 ? 2 + t / 3 : -1);
          if (k >= 0) wv[k] = ld_nt(rv, vo, (64 * w + k) * 8192 + h * 256 + dh * 128); }
      }
      SB();
    }
  }
  float m[2];
#pragma unroll
  for (int hg = 0; hg < 2; ++hg) {
    float t = fmaxf(fmaxf(fmaxf(d[0][hg][0], d[0][hg][1]), fmaxf(d[0][hg][2], d[0][hg][3])), fmaxf(fmaxf(d[1][hg][0], d[1][hg][1]), fmaxf(d[1][hg][2], d[1][hg][3])));
    m[hg] = x32_max(x16_max(t));
  }
  constexpr int PST = 528, MX = 17408, LS = 18432, WT = 19456;
  bar();
  STAMP(5);
  if (g < 2) *LP(float, MX + ((16 * g + c) * 8 + w) * 4) = g ? m[1] : m[0];
  bar();
  if ((PRE & 4) && w == 6 && !(i & 1)) dma_sc(ws, lane * 16, WS_AML + (b * 32 + sp) * 512, 24576);
  if (PRE & 8) dma_sc(ws, lane * 16, WS_OPA + (x * 8 + i) * 8192 + w * 1024, 24576);
  wv[7] = ld_nt(rv, vo, (64 * w + 7) * 8192 + h * 256 + dh * 128);
#pragma unroll
  for (int hg = 0; hg < 2; ++hg) {
    const f32x4 a0 = *LP(f32x4, MX + (16 * hg + c) * 32), a1 = *LP(f32x4, MX + (16 * hg + c) * 32 + 16);
    m[hg] = fmaxf(fmaxf(fmaxf(a0[0], a0[1]), fmaxf(a0[2], a0[3])), fmaxf(fmaxf(a1[0], a1[1]), fmaxf(a1[2], a1[3])));
  }
  float l[2];
#pragma unroll
  for (int hg = 0; hg < 2; ++hg) {
    const float mc = -m[hg] * C2;
    float s = 0.f;
#pragma unroll
    for (int ab = 0; ab < 2; ++ab) {
      float e[4];
#pragma unroll
      for (int r = 0; r < 4; ++r) { e[r] = ex2(fmaf(d[ab][hg][r], C2, mc)); s += e[r]; }
      *LP(v2u, (16 * hg + c) * PST + (128 * ab + 16 * w + 4 * g) * 2) = v2u{pk(e[0], e[1]), pk(e[2], e[3])};
    }
    l[hg] = x32_sum(x16_sum(s));
  }
  if (g < 2) *LP(float, LS + ((16 * g + c) * 8 + w) * 4) = g ? l[1] : l[0];
  const float Mh = (g & 1) ? m[1] : m[0];
  bar();
  STAMP(6);
  f32x16 o[2];
#pragma unroll
  for (int k = 0; k < 16; ++k) { o[0][k] = 0.f; o[1][k] = 0.f; }
  {
    int pb = (lane & 31) * PST + 16 * hh;
    PIN(pb); PIN(ta[0][0]); PIN(ta[0][1]); PIN(ta[1][0]); PIN(ta[1][1]);
    v4u pf[3], tb[3][2];
#pragma unroll
    for (int s = 0; s < 18; ++s) {
      if (s < 16) { pf[s % 3] = lds128(pb + 32 * s); if (s >= 8) { tb[s % 3][0] = trfrag(0, s - 8); tb[s % 3][1] = trfrag(1, s - 8); } }
      SB();
      if (s >= 2) {
        const int t = s - 2;
        o[0] = mfma32(t < 8 ? pa[t][0] : tb[t % 3][0], pf[t % 3], o[0]);
        o[1] = mfma32(t < 8 ? pa[t][1] : tb[t % 3][1], pf[t % 3], o[1]);
        if (w < 4 && !(t & 1) && (t >> 1) < WO_EARLY) wo[t >> 1] = ld_wo(t >> 1);
      }
      SB();
    }
  }
  float Lh;
  { const f32x4 a0 = *LP(f32x4, LS + (lane & 31) * 32), a1 = *LP(f32x4, LS + (lane & 31) * 32 + 16); Lh = ((a0[0] + a0[1]) + (a0[2] + a0[3])) + ((a1[0] + a1[1]) + (a1[2] + a1[3])); }
  bar();
  STAMP(7);
  {
    const lds_t stg = IMG + w * 4608;
#pragma unroll
    for (int ct = 0; ct < 2; ++ct)
#pragma unroll
      for (int rq = 0; rq < 4; ++rq)
        *LP(v2u, stg + (lane & 31) * 144 + (32 * ct + 8 * rq + 4 * hh) * 2) = v2u{pk(o[ct][4 * rq], o[ct][4 * rq + 1]), pk(o[ct][4 * rq + 2], o[ct][4 * rq + 3])};
    asm volatile("" ::: "memory");
    v4u v[4];
#pragma unroll
    for (int e = 0; e < 4; ++e) v[e] = lds128(stg + (8 * e + (lane >> 3)) * 144 + (lane & 7) * 16);
    SB();
#pragma unroll
    for (int e = 0; e < 4; ++e) {
      st_sc(v[e], ws, (lane >> 3) * 65536 + (lane & 7) * 16, WS_AO + e * 8 * 65536 + (b * 32 + sp) * 1024 + w * 128);
    }
  }
  VMCNT(0);
  bar();
  STAMP(8);
  if (w == 7 && lane < 32) st_sc(v4u{fu(Mh), tag, fu(Lh), tag}, ws, lane * 16, WS_AML + (b * 32 + sp) * 512);
  if (!TWA && w < 4) {
#pragma unroll
    for (int n = WO_EARLY; n < 16; ++n) wo[n] = ld_wo(n);
  }
  SB();
  transpose8(wv);
#pragma unroll
  for (int n = 0; n < 8; ++n) PIN(wv[n]);
  constexpr int XS = IMG + 40960, G1 = XS + 8448, YS = G1 + 8192;
  {
    v4u f;
    if (D_AML) __builtin_amdgcn_s_sleep(D_AML);
    gather<1, SL_AML>(&f, ws, (lane & 7) * 512, WS_AML + w * 4096 + h * 16, tag);
    STAMP(9);
    v4u t[8];
#pragma unroll
    for (int k = 0; k < 8; ++k) t[k] = ld_sc(ws, lane * 16 + (k & 3) * 1024, WS_AO + h * 65536 + w * 8192 + (k >> 2) * 4096);
    SB();
    const float mm = uf(f[0]);
    float M = fmaxf(mm, dpp<XOR1>(mm)); M = fmaxf(M, dpp<XOR2>(M)); M = fmaxf(M, dpp<ROR4>(M));
    const float e = ex2((mm - M) * C2);
    float Lw = e * uf(f[2]); Lw += dpp<XOR1>(Lw); Lw += dpp<XOR2>(Lw); Lw += dpp<ROR4>(Lw);
    if (lane == 0) *LP(f32x2, WT + w * 8) = f32x2{M, Lw};
    if (!TWA && w < 4) { transpose8(wo); transpose8(wo + 8); }
    float xs[8];
#pragma unroll
    for (int j = 0; j < 8; ++j) xs[j] = 0.f;
#pragma unroll
    for (int k = 0; k < 8; ++k) {
      const float wt = uf(__builtin_amdgcn_readlane(fu(e), k));
#pragma unroll
      for (int j = 0; j < 4; ++j) { xs[2 * j] = fmaf(wt, bflo(t[k][j]), xs[2 * j]); xs[2 * j + 1] = fmaf(wt, bfhi(t[k][j]), xs[2 * j + 1]); }
    }
    *LP(v4u, XS + w * 1056 + lane * 16) = v4u{pk(xs[0], xs[1]), pk(xs[2], xs[3]), pk(xs[4], xs[5]), pk(xs[6], xs[7])};
  }
  bar();
  STAMP(10);
  if (TWA && w < 4) {
    pace(t0, TWA);
#pragma unroll
    for (int n = WO_EARLY; n < 16; ++n) wo[n] = ld_wo(n);
    SB();
  }
  {
    const v4u xa = lds128(XS + (4 * (c >> 3) + (c & 3)) * 1056 + (8 * w + 2 * g + ((c >> 2) & 1)) * 16);
    const f32x4 m0 = *LP(f32x4, WT + hh * 32), m1 = *LP(f32x4, WT + hh * 32 + 16);
    const float M = fmaxf(fmaxf(m0[0], m0[2]), fmaxf(m1[0], m1[2]));
    f32x4 fs = {ex2((m0[0] - M) * C2), ex2((m0[2] - M) * C2), ex2((m1[0] - M) * C2), ex2((m1[2] - M) * C2)};
    fs *= 1.0f / (fs[0] * m0[1] + fs[1] * m0[3] + fs[2] * m1[1] + fs[3] * m1[3]);
    float y[8];
#pragma unroll
    for (int jj = 0; jj < 8; ++jj) { const f32x4 dd = mfma16(xa, wv[jj], f32x4{0.f, 0.f, 0.f, 0.f}); y[jj] = (fs[0] * dd[0] + fs[1] * dd[1]) + (fs[2] * dd[2] + fs[3] * dd[3]); }
    if (((lane >> 3) & 1) == (g & 1)) {
      const lds_t dst = G1 + ((2 * w + (g & 1)) * 128 + (lane >> 5) * 64 + 8 * (lane & 7)) * 4;
      *LP(f32x4, dst) = f32x4{y[0], y[1], y[2], y[3]};
      *LP(f32x4, dst + 16) = f32x4{y[4], y[5], y[6], y[7]};
    }
  }
  bar();
  STAMP(11);
  if (w < 4) {
    f32x2 s = {0.f, 0.f};
#pragma unroll
    for (int k = 0; k < 16; ++k) s += *LP(f32x2, G1 + k * 512 + lane * 8);
    const lds_t ys = YS + w * 256;
    *LP(unsigned, ys + lane * 4) = pk(s[0], s[1]);
    asm volatile("" ::: "memory");
    if (TWA) { transpose8(wo); transpose8(wo + 8); }
    out_gemv<2>(wo, ys, ws, WS_OPA + w * 16 * 8192 + (2 * h + dh) * 128, 8192, tag, lane);
  } else if (w == 4) {
    const int slice = x * 8 + i;
    final_reduce<8>(ws, WS_OPA + slice * 8192, slice, p.out, 0, tag, lane);
  } else if (w == 5 && x == 0 && i == 0) {
    for (unsigned spins = 0;; ++spins) {
      const v4u f = ld_sc(ws, lane * 256, WS_QB);
      if (all_ok(tags_ok(f, tag)) || spins >= SPIN_CAP) break;
      asm volatile("" ::: "memory");
      __builtin_amdgcn_s_sleep(1);
    }
    if (lane == 0) __builtin_amdgcn_raw_buffer_store_b32(tag, ws, 0, WS_EPOCH, AUX_SC1);
  }
  STAMP(12);
  STAMP_FLUSH();
}

constexpr int B_X = 65536, B_QB = 67584, B_QA = 68608, B_ML = 69120, B_E = 69376, B_CNT = 69632, B_WT = 69888, B_EP = 70656, B_KS = 73728;
#define ST_ARG
#define ST_PASS

DI void k_write(const v4u& kr, int j, lds_t my, int c, int g) { const int key = 4 * j + g; *LP(v4u, my + key * 256 + ((c ^ (key & 15)) << 4)) = kr; }
DI void k_read(v4u* kr, lds_t my, int c, int g) {
  const int k0 = 4 * (c & 3) + (c >> 2);
#pragma unroll
  for (int T = 0; T < 2; ++T)
#pragma unroll
    for (int s = 0; s < 4; ++s) kr[4 * T + s] = lds128(my + (16 * T + k0) * 256 + (((4 * s + g) ^ k0) << 4));
}
DI unsigned lds_arrive(lds_t cnt, int lane) {
  unsigned old = 0;
  asm volatile("s_waitcnt lgkmcnt(0)" ::: "memory");
  if (lane == 0) old = __hip_atomic_fetch_add(LP(unsigned, cnt), 1u, __ATOMIC_RELAXED, __HIP_MEMORY_SCOPE_WORKGROUP);
  old = __builtin_amdgcn_readfirstlane(old);
  asm volatile("" ::: "memory");
  return old;
}
template <int NU> DI void b_scores(const v4u* kr, v4u* pf, rsrc_t ws, int w, int lane, int nwaves, int mlbase, unsigned tag) {
  const int c = lane & 15, g = lane >> 4;
  v4u qf[4];
#pragma unroll
  for (int s = 0; s < 4; ++s) qf[s] = lds128(B_QB + (c >> 2) * 256 + 64 * s + 16 * g);
  float sc[8 * NU];
#pragma unroll
  for (int T = 0; T < 2 * NU; ++T) {
    f32x4 d = {0.f, 0.f, 0.f, 0.f};
#pragma unroll
    for (int s = 0; s < 4; ++s) d = mfma16(kr[4 * T + s], qf[s], d);
#pragma unroll
    for (int r = 0; r < 4; ++r) sc[4 * T + r] = d[r];
  }
  float m = sc[0];
#pragma unroll
  for (int k = 1; k < 8 * NU; ++k) m = fmaxf(m, sc[k]);
  m = x32_max(x16_max(m));
  float l = 0.f;
  const float mc = -m * C2;
#pragma unroll
  for (int k = 0; k < 8 * NU; ++k) { sc[k] = ex2(fmaf(sc[k], C2, mc)); l += sc[k]; }
  l = x32_sum(x16_sum(l));
#pragma unroll
  for (int u = 0; u < NU; ++u)
#pragma unroll
    for (int t = 0; t < 4; ++t) pf[u][t] = pk(sc[8 * u + 2 * t], sc[8 * u + 2 * t + 1]);
  if (lane < 16 && (c & 3) == 0) *LP(f32x2, B_ML + (w * 4 + (c >> 2)) * 8) = f32x2{m, l};
  if ((int)lds_arrive(B_CNT, lane) == nwaves - 1) {
    const f32x2 ml = *LP(f32x2, B_ML + (lane & 31) * 8);
    float M = ml[0];
    M = fmaxf(M, dpp<ROR4>(M)); M = fmaxf(M, dpp<ROR8>(M)); M = x16_max(M);
    const float e = ex2((ml[0] - M) * C2);
    float t = ml[1] * e;
    t += dpp<ROR4>(t); t += dpp<ROR8>(t); t = x16_sum(t);
    if (lane < 32) *LP(float, B_E + ((lane & 3) * 8 + (lane >> 2)) * 4) = e;
    if (lane < 8) st_sc(v4u{fu(M), tag, fu(t), tag}, ws, lane * 16, mlbase);
  }
}
DI void b_pv(const v4u& pf, const v4u* vr, f32x4* acc) {
  v4u t[8];
#pragma unroll
  for (int j = 0; j < 4; ++j) {
#pragma unroll
    for (int k = 0; k < 8; ++k) t[k][j] = __builtin_amdgcn_perm(vr[2 * j + 1][k >> 1], vr[2 * j][k >> 1], (k & 1) ? 0x07060302u : 0x05040100u);
    SB();
  }
#pragma unroll
  for (int k = 0; k < 8; ++k) acc[k] = mfma16(pf, t[k], acc[k]);
#pragma unroll
  for (int k = 0; k < 8; ++k) PIN(acc[k]);
}
DI void b_merge(const f32x4* acc, rsrc_t ws, int w, int lane, int nwaves, int obase, unsigned tag ST_ARG) {
  const lds_t mo = B_KS + w * 8192 + lane * 32;
  *LP(f32x4, mo) = f32x4{acc[0][0], acc[1][0], acc[2][0], acc[3][0]};
  *LP(f32x4, mo + 16) = f32x4{acc[4][0], acc[5][0], acc[6][0], acc[7][0]};
  if ((int)lds_arrive(B_CNT + 4, lane) == nwaves - 1) {
    int ob = B_KS + lane * 16, eb = B_E + (lane >> 5) * 32;
    PIN(ob); PIN(eb);
    f32x4 e[2][2], v[2][8];
#pragma unroll
    for (int eh = 0; eh < 2; ++eh) {
      e[eh][0] = *LP(f32x4, eb + eh * 64); e[eh][1] = *LP(f32x4, eb + eh * 64 + 16);
#pragma unroll
      for (int k = 0; k < 8; ++k) v[eh][k] = *LP(f32x4, ob + k * 8192 + eh * 1024);
    }
    SB();
#pragma unroll
    for (int eh = 0; eh < 2; ++eh) {
      f32x4 s = e[eh][0][0] * v[eh][0];
#pragma unroll
      for (int k = 1; k < 8; ++k) s += e[eh][k >> 2][k & 3] * v[eh][k];
      st_sc(v4u{pk(s[0], s[1]), tag, pk(s[2], s[3]), tag}, ws, (lane >> 5) * 12288 + (lane & 31) * 16, obase + eh * 2 * 12288);
    }
    STAMP(8);
  }
}
DI void lds_signal(lds_t cnt, int lane) { if (lane == 0) (void)__hip_atomic_fetch_add(LP(unsigned, cnt), 1u, __ATOMIC_RELAXED, __HIP_MEMORY_SCOPE_WORKGROUP); }
DI void lds_wait(lds_t cnt, unsigned want) {
  for (unsigned spins = 0; *(volatile LAS unsigned*)(size_t)cnt < want && spins < LSPIN; ++spins) __builtin_amdgcn_s_sleep(1);
  asm volatile("" ::: "memory");
}
template <int NCT> DI void gemv_n(f32x4* d, int ct0, lds_t wb, lds_t xb, int c, int g) {
  const int q4 = c >> 2, p4 = c & 3, fx = (q4 >> 1) | ((g & 1) << 1);
  int a[NCT];
#pragma unroll
  for (int ct = 0; ct < NCT; ++ct) { a[ct] = wb + (8 * g + q4) * 128 + (((ct0 + ct) ^ fx) << 5) + 8 * p4; PIN(a[ct]); }
  int xa = xb + (c & 1) * 512 + 16 * g;
  PIN(xa);
  constexpr int D = NCT == 1 ? 4 : 1;
  v4u xf[D + 1], wf[D + 1][NCT];
#pragma unroll
  for (int s = 0; s < 8 + D; ++s) {
    if (s < 8) {
      xf[s % (D + 1)] = lds128(xa + 64 * s);
#pragma unroll
      for (int ct = 0; ct < NCT; ++ct) { const v2u r0 = ldstr(a[ct] + 4096 * s), r1 = ldstr(a[ct] + 4096 * s + 512); wf[s % (D + 1)][ct] = v4u{r0[0], r0[1], r1[0], r1[1]}; }
    }
    SB();
    if (s >= D) {
#pragma unroll
      for (int ct = 0; ct < NCT; ++ct) d[ct] = mfma16(wf[(s - D) % (D + 1)][ct], xf[(s - D) % (D + 1)], d[ct]);
    }
    SB();
  }
}

template <int NU, int LS, int WIN>
DI void b_stream(unsigned t0, rsrc_t rk, rsrc_t rv, rsrc_t ws, int ub, int w, int lane, int nwaves, int obase, int mlbase ST_ARG) {
  const int c = lane & 15, g = lane >> 4;
  const lds_t my = B_KS + w * 8192;
  constexpr int NL = 8 * NU;
  auto off = [&](int n) { return NU == 1 ? ub + n * LS : ub + (n / GI) * (12 * GI * LS) + (n % GI) * LS; };
  v4u kr[NL], vr[NL], pf[NU];
  f32x4 acc[8];
#pragma unroll
  for (int k = 0; k < 8; ++k) acc[k] = f32x4{0.f, 0.f, 0.f, 0.f};
#pragma unroll
  for (int n = 0; n < NL + WQK; ++n) {
    if (n < WIN) vmcnt<WIN - 1>();
    if (PB && NU > 1 && n >= 8 && n % PSTEP == 0) pace(t0, PA + n * PB / 8);
    if (n < NL) kr[n] = ld_nt(rk, lane * 16, off(n));
    else vr[n - NL] = ld_nt(rv, lane * 16, off(n - NL));
    SB();
    const int t = n + 1 - WIN;
    if (t >= 0 && t < NL) { k_write(kr[t], t & 7, my, c, g); if ((t & 7) == 7) k_read(kr + (t - 7), my, c, g); }
    SB();
  }
  STAMP(3);
  lds_wait(B_CNT + 8, 1u);
  const unsigned tag = __builtin_amdgcn_readfirstlane(*LP(unsigned, B_EP)) + 1u;
  STAMP(4);
  b_scores<NU>(kr, pf, ws, w, lane, nwaves, mlbase, tag);
  SB();
  STAMP(5);
#pragma unroll
  for (int n = WQK; n < NL; ++n) {
    vmcnt<WIN - 1>();
    if (PB && n % PSTEP == 0) pace(t0, PA + (NL + n) * PB / 8);
    vr[n] = ld_nt(rv, lane * 16, off(n));
    SB();
    if (n == 13) { b_pv(pf[0], vr, acc); SB(); }
  }
  STAMP(6);
  if (NU > 1) lds_signal(B_CNT + 24, lane);
  if (NU > 1) { b_pv(pf[NU - 2], vr + 8 * (NU - 2), acc); SB(); }
  b_pv(pf[NU - 1], vr + 8 * (NU - 1), acc);
  SB();
  STAMP(7);
  b_merge(acc, ws, w, lane, nwaves, obase, tag ST_PASS);
}
DI void fetch_qb(unsigned t0, rsrc_t ws, int src, unsigned tag, int lane) {
  if (QBT) pace(t0, QBT);
  v4u f[2];
  gather<2, 4>(f, ws, lane * 16, src, tag);
  const v4u f0 = f[0], f1 = f[1];
  *LP(v2u, B_QB + lane * 8) = v2u{f0[0], f0[2]};
  *LP(v2u, B_QB + 512 + lane * 8) = v2u{f1[0], f1[2]};
  asm volatile("s_waitcnt lgkmcnt(0)" ::: "memory");
  if (lane == 0) *(volatile LAS unsigned*)(size_t)(B_CNT + 8) = 1u;
}

DI void role_b(const Params& p, int x, int j, int lane, int w) {
  STAMP_DECL
  STAMP(0);
  const unsigned t0 = now();
  const int type = j % 3, k3 = j / 3, b = j / 12, sp = j % 12;
  const bool tq = type != 2;
  const int c = lane & 15, g = lane >> 4;
  const int col = 8 * k3 + ((x + k3) & 7);
  const rsrc_t ws = mk(p.ws, WS_BYTES);
  const rsrc_t rw = mk(tq ? p.wqa : p.wqb, 512 * 8192);
  const int sb = (type & 1) * 256 * 8192 + col * 128;
  const int vn0 = (lane >> 3) * 8192 + (((lane & 7) ^ ((g & 1) << 1)) << 4), vn1 = vn0 ^ 64;
  auto dma_w = [&](int n) {
    dma_nt(rw, (n & 1) ? vn1 : vn0, sb + ((n >> 2) * 256 + 32 * w + 8 * (n & 3)) * 8192, (n >> 2) * 32768 + (4 * w + (n & 3)) * 1024);
  };
  const int hq = col >> 1, hu = (hq & ~3) | ((hq + 2) & 3), cq = 2 * (col & 1) + type;
  constexpr int DUMMY = 71680;
  if (tq) {
#pragma unroll
    for (int n = 0; n < 4; ++n) dma_w(n);
  } else {
    const int ka = (3 - x - ((unsigned)(size_t)p.wqa >> 7)) & 7, kb = (3 - x - ((unsigned)(size_t)p.wqb >> 7)) & 7;
    const int vp = (lane >> 3) * 8192 + (lane & 7) * 16, sp0 = (8 * k3 + w) * 8 * 8192;
    dma_df(mk(p.wqa, 512 * 8192), vp, sp0 + (8 * ka + ((x + ka) & 7)) * 128, DUMMY);
    dma_df(rw, vp, sp0 + (8 * kb + ((x + kb) & 7)) * 128, DUMMY);
    dma_w(0); dma_w(1);
  }
  if (w == 0) {
    const rsrc_t rx = mk(tq ? p.xa : p.xb, 2048);
    const int vx = (lane >> 5) * 1024 + (lane & 31) * 16;
    dma_df(rx, vx, (type & 1) * 512, B_X); dma_df(rx, vx, 512, B_X + 1024);
    dma_sc(ws, 0, WS_EPOCH, B_EP);
    if ((PRE & 1) && tq) dma_sc(ws, lane * 16, WS_QA + hu * 4096 + cq * 1024, DUMMY);
    if ((PRE & 1) && !tq && k3 == 0) dma_sc(ws, lane * 16, WS_QFLAG, DUMMY);
    if (lane < 8) *LP(unsigned, B_CNT + lane * 4) = 0u;
    if (lane < 4) *LP(f32x2, B_ML + lane * 8) = f32x2{-INFINITY, 0.f};
    *LP(f32x4, B_KS + lane * 32) = f32x4{0.f, 0.f, 0.f, 0.f}; *LP(f32x4, B_KS + lane * 32 + 16) = f32x4{0.f, 0.f, 0.f, 0.f};
  }
  const rsrc_t rk = mk(p.kc, 16 * 8192 * 256), rv = mk(p.vc, 16 * 8192 * 256);
  const int pbase = (b * 8 + x) * 8192 * 256;
  const int obase = WS_BO + 4 * x * 12288 + sp * 1024 + b * 512, mlbase = WS_BML + x * 4096 + b * 2048 + sp * 128;
  if (!tq) {
    bar();
#pragma unroll
    for (int n = 2; n < 8; ++n) { VMCNT(4); dma_w(n); }
    VMCNT(4);
    if (w > 0) {
      lds_signal(B_CNT + 12, lane);
      STAMP(1);
      const rsrc_t rc = mk(p.ckv, 2 * 8192 * 1024);
      const int cb = ((x & 1) * 8192 + (x >> 1) * 2048 + k3 * 256) * 1024;
#pragma unroll
      for (int s = 0; s < PFN; ++s) {
        VMCNT(4);
        if (s == 4) { lds_signal(B_CNT + 16, lane); STAMP(2); }
        dma_df(rc, lane * 16, cb + ((256 - 7 * PFN + 7 * s + w - 1 + 32 * k3) & 255) * 1024, DUMMY);
      }
    }
    STAMP(12);
    {
      int cs = 0;
#pragma unroll
      for (int i = 0; i < 8; ++i) { const int cc = 2 * (4 * ((i & 6) + (x & 1)) + (((x >> 1) + (i >> 1)) & 3)) + (i & 1); if (((cc + ((unsigned)(size_t)p.wuv >> 7)) & 7) == 3) cs = cc; }
      if (w > 0) dma_df(mk(p.wuv, 512 * 8192), (lane >> 3) * 8192 + (lane & 7) * 16, (8 * k3 + w) * 8 * 8192 + cs * 128, DUMMY);
    }
  } else {
    bar();
    const rsrc_t ru = mk(p.wuk, 512 * 8192);
    const int vt = g * 8192 + ((c ^ (g | ((w & 1) << 3))) << 4);
#pragma unroll
    for (int n = 0; n < 4; ++n) { VMCNT(3); dma_nt(ru, vt ^ ((n >> 1) << 6), (cq * 128 + 16 * w + 4 * n) * 8192 + hu * 256, 32768 + (4 * w + n) * 1024); }
    VMCNT(4);
    bar();
    STAMP(1);
    if (w >= 4) {
      const unsigned tag = __builtin_amdgcn_readfirstlane(*LP(unsigned, B_EP)) + 1u;
      f32x4 d = {0.f, 0.f, 0.f, 0.f};
      gemv_n<1>(&d, w - 4, 0, B_X, c, g);
      if (c < 4) st_sc(v4u{fu(c < 2 ? d[0] : d[2]), tag, fu(c < 2 ? d[1] : d[3]), tag}, ws, (c & 1) * 1024 + g * 32 + (c >> 1) * 16, WS_QA + hq * 4096 + type * 2048 + (col & 1) * 512 + (w - 4) * 128);
    }
    VMCNT(0);
    bar();
    STAMP(12);
    if (w > 0) { lds_wait(B_CNT + 20, 1u); STAMP(9); }
  }
  if (w > 0) {
    b_stream<3, 7168, W1>(t0, rk, rv, ws, pbase + sp * GI * 7168 + (w - 1) * 1024, w, lane, tq ? 7 : 8, obase, mlbase ST_PASS);
    STAMP_FLUSH();
    return;
  }
  if (!tq) {
    lds_wait(B_CNT + 12, 7u);
    STAMP(1);
    const unsigned tag = __builtin_amdgcn_readfirstlane(*LP(unsigned, B_EP)) + 1u;
    f32x4 d[4];
#pragma unroll
    for (int ct = 0; ct < 4; ++ct) d[ct] = f32x4{0.f, 0.f, 0.f, 0.f};
#pragma unroll 1
    for (int part = 0; part < 2; ++part) {
      if (part) { VMCNT(0); lds_wait(B_CNT + 16, 7u); STAMP(2); }
      gemv_n<4>(d, 0, part * 32768, B_X + part * 1024, c, g);
    }
    const int ct = (c >> 1) & 3;
    const f32x4 dd = ct == 0 ? d[0] : ct == 1 ? d[1] : ct == 2 ? d[2] : d[3];
    if (c < 8) st_sc(v4u{pk(dd[0], dd[1]), tag, pk(dd[2], dd[3]), tag}, ws, (c & 1) * 16384 + ct * 64 + g * 16, WS_QB + col * 256);
    STAMP(11);
    fetch_qb(t0, ws, WS_QB + (b * 8 + x) * 2048, tag, lane);
    STAMP(13);
    if (PRE & 16) {
#pragma unroll
      for (int n = 0; n < 4; ++n) dma_sc(ws, lane * 16, WS_OPB + (8 * x + k3) * 4096 + n * 1024, DUMMY);
    }
    b_stream<1, 1024, WCTL>(t0, rk, rv, ws, pbase + (252 + (k3 & 3)) * 8192, 0, lane, 8, obase, mlbase ST_PASS);
    lds_wait(B_CNT + 4, 8u);
    STAMP(9);
    const int slice = 8 * x + k3;
    final_reduce<4>(ws, WS_OPB + slice * 4096, slice, p.out, 512, tag, lane);
    STAMP(10);
    STAMP_FLUSH();
    return;
  }
  const unsigned tag = __builtin_amdgcn_readfirstlane(*LP(unsigned, B_EP)) + 1u;
  {
    v4u wf[32];
    {
      const int r0 = 32768 + (8 * (c >> 2) + (c & 3)) * 256;
#pragma unroll
      for (int s = 0; s < 4; ++s) {
        int a = r0 + (((4 * s + g) ^ c) << 4);
        PIN(a);
#pragma unroll
        for (int u = 0; u < 8; ++u) wf[4 * u + s] = lds128(a + (u >> 1) * 8192 + (u & 1) * 1024);
      }
    }
    v4u f[4];
    gather<4, 0>(f, ws, lane * 16, WS_QA + hu * 4096, tag);
    STAMP(2);
    *LP(unsigned, B_QA + lane * 4) = pk(uf(f[0][0]) + uf(f[2][0]), uf(f[0][2]) + uf(f[2][2]));
    *LP(unsigned, B_QA + 256 + lane * 4) = pk(uf(f[1][0]) + uf(f[3][0]), uf(f[1][2]) + uf(f[3][2]));
    v4u qf[4];
#pragma unroll
    for (int s = 0; s < 4; ++s) qf[s] = lds128(B_QA + (c & 1) * 256 + 64 * s + 16 * g);
    f32x4 e[8];
#pragma unroll
    for (int u = 0; u < 8; ++u) e[u] = f32x4{0.f, 0.f, 0.f, 0.f};
#pragma unroll
    for (int s = 0; s < 4; ++s)
#pragma unroll
      for (int u = 0; u < 8; ++u) e[u] = mfma16(wf[4 * u + s], qf[s], e[u]);
    const int ct = (c >> 1) & 3;
    const f32x4 d0 = ct == 0 ? e[0] : ct == 1 ? e[2] : ct == 2 ? e[4] : e[6], d1 = ct == 0 ? e[1] : ct == 1 ? e[3] : ct == 2 ? e[5] : e[7];
    if (c < 8) st_sc(v4u{pk(d0[0], d0[1]), pk(d0[2], d0[3]), pk(d1[0], d1[1]), pk(d1[2], d1[3])}, ws, (c & 1) * 32768 + ct * 64 + g * 16, WS_QABS + hu * 1024 + cq * 256);
    STAMP(3);
    VMCNT(0);
    if (lane == 0) __builtin_amdgcn_raw_buffer_store_b64(v2u{tag, ~tag}, ws, WS_QFLAG + hu * FSTR + cq * 8, 0, AUX_SC1);
    STAMP(4);
    if (lane == 0) *(volatile LAS unsigned*)(size_t)(B_CNT + 20) = 1u;
  }
  for (int k = 0; k < QBDELAY; ++k) __builtin_amdgcn_s_sleep(8);
  STAMP(5);
  fetch_qb(t0, ws, WS_QB + (b * 8 + x) * 2048, tag, lane);
  STAMP(6);
  {
    const int k2 = 2 * k3 + type, h = 4 * x + (k2 >> 2), cg = k2 & 3;
    const rsrc_t ro = mk(p.wob, 4096 * 1024);
    if (PRE & 2) {
#pragma unroll
      for (int n = 0; n < 3; ++n) dma_sc(ws, lane * 16, WS_BO + h * 12288 + (3 * cg + n) * 1024, DUMMY);
    }
    v4u wo[32];
    auto ld_wo = [&](int n) { return ld_nt(ro, g * 8192 + c * 16, (h * 128 + 32 * (n >> 3) + (n & 7)) * 1024 + cg * 256); };
#pragma unroll
    for (int n = 0; n < WO1; ++n) { wo[n] = ld_wo(n); vmcnt<WCTL - 1>(); SB(); }
#pragma unroll
    for (int t = 0; t < WO1 / 8; ++t) transpose8(wo + 8 * t);
#pragma unroll
    for (int n = 0; n < WO1; ++n) PIN(wo[n]);
    STAMP(7);
    lds_wait(B_CNT + 24, WO_LATE);
    float wt[12];
    {
      const bool act = (lane & 31) < 12;
      const int mo = (lane >> 5) * 2048 + (act ? (lane & 31) : 0) * 128;
      v4u f = ld_sc(ws, mo, WS_BML + x * 4096 + (h & 3) * 16);
      SB();
      if (TWO) pace(t0, TWO);
#pragma unroll
      for (int n = WO1; n < 32; ++n) wo[n] = ld_wo(n);
      SB();
      for (unsigned spins = 0; !all_ok(tags_ok(f, tag)) && spins < SPIN_CAP; ++spins) {
        asm volatile("" ::: "memory");
        __builtin_amdgcn_s_sleep(4);
        f = ld_sc(ws, mo, WS_BML + x * 4096 + (h & 3) * 16);
      }
      const float mm = uf(f[0]), M = row_max(mm);
      const float e = act ? ex2((mm - M) * C2) : 0.f, Lt = row_sum(e * uf(f[2]));
      if ((lane & 16) == 0) *LP(float, B_WT + ((lane >> 5) * 16 + c) * 4) = e / Lt;
      asm volatile("" ::: "memory");
#pragma unroll
      for (int s = 0; s < 3; ++s) {
        const f32x4 t = *LP(f32x4, B_WT + (lane >> 5) * 64 + s * 16);
        wt[4 * s] = t[0]; wt[4 * s + 1] = t[1]; wt[4 * s + 2] = t[2]; wt[4 * s + 3] = t[3];
      }
    }
    STAMP(8);
    if (WO1 < 32) {
#pragma unroll
      for (int t = WO1 / 8; t < 4; ++t) transpose8(wo + 8 * t);
#pragma unroll
      for (int n = WO1; n < 32; ++n) PIN(wo[n]);
    }
    STAMP(11);
    lds_wait(B_CNT + 4, 7u);
    v4u v[12];
    gather<12, SL_BO>(v, ws, lane * 16, WS_BO + h * 12288, tag);
    STAMP(13);
    float o[4] = {0.f, 0.f, 0.f, 0.f};
#pragma unroll
    for (int s = 0; s < 12; ++s) {
      o[0] = fmaf(wt[s], bflo(v[s][0]), o[0]); o[1] = fmaf(wt[s], bfhi(v[s][0]), o[1]);
      o[2] = fmaf(wt[s], bflo(v[s][2]), o[2]); o[3] = fmaf(wt[s], bfhi(v[s][2]), o[3]);
    }
    STAMP(9);
    *LP(v2u, B_QA + lane * 8) = v2u{pk(o[0], o[1]), pk(o[2], o[3])};
    asm volatile("" ::: "memory");
    out_gemv<4>(wo, B_QA, ws, WS_OPB + cg * 16 * 4096 + h * 128, 4096, tag, lane);
    STAMP(10);
  }
  STAMP_FLUSH();
}

__global__ void __launch_bounds__(512, 2) k_fused(Params p) {
  const int lane = threadIdx.x & 63, w = __builtin_amdgcn_readfirstlane(threadIdx.x >> 6);
  const int x = blockIdx.x & 7, i = blockIdx.x >> 3;
  if (w >= 6 && i + 32 * (7 - w) < CODE_KB) {
    const rsrc_t rcode = mk((const void*)(__builtin_amdgcn_s_getpc() & ~255ull), CODE_KB * 1024);
    dma_df(rcode, lane * 16, (i + 32 * (7 - w)) * 1024, i >= 24 ? 2048 : 71680);
  }
  if (i >= 24) role_a(p, x, i - 24, lane, w);
  else role_b(p, x, i, lane, w);
}

extern "C" void kernel_launch(void* const* d_in, const int* in_sizes, int n_in, void* d_out, int out_size, void* d_ws, size_t ws_size, hipStream_t stream) {
  Params p;
  p.xa = d_in[0]; p.ckv = d_in[1]; p.wqa = d_in[2]; p.wuk = d_in[3]; p.wuv = d_in[4]; p.woa = d_in[5];
  p.xb = d_in[6]; p.kc = d_in[7]; p.vc = d_in[8]; p.wqb = d_in[9]; p.wob = d_in[10];
  p.out = d_out; p.ws = d_ws;
  (void)hipFuncSetAttribute((const void*)k_fused, hipFuncAttributeMaxDynamicSharedMemorySize, 163840);
  k_fused<<<256, 512, 163840, stream>>>(p);
}
```
